```python
import torch

def _softmax(x, axis=-1):
    return torch.softmax(x, dim=axis)

def _gelu(x):
    return torch.nn.functional.gelu(x, approximate='tanh')

def _silu(x):
    return torch.nn.functional.silu(x)

def _sigmoid(x):
    return torch.sigmoid(x)

def _layernorm(x, axis=-1, eps=1e-05):
    m = x.mean(dim=axis, keepdim=True)
    v = x.var(dim=axis, keepdim=True, unbiased=False)
    return (x - m) / torch.sqrt(v + eps)

def _scatter_add(tgt, idx, src, axis=0):
    return tgt.clone().index_add_(axis, idx, src)

def _segment_sum(src, seg, n):
    out = torch.zeros((n,) + src.shape[1:], dtype=src.dtype)
    return out.index_add_(0, seg, src)

def _pad(x, pw):
    flat = tuple((v for pair in reversed(pw) for v in pair))
    return torch.nn.functional.pad(x, flat)
kernel_name = 'parallel_sum__memory_aug_recurrent_mem_tr__mixer_gmlp__prompt'

def setup_inputs():

    _w = lambda a: (lambda t: t.to(torch.bfloat16) if t.is_floating_point() else t)(torch.as_tensor(a))

    def _A_setup():
        B, S, Dm = (1, 2048, 512)
        H, Dh = (32, 128)
        M, seg = (8, 32)
        seg = min(seg, S)
        S = S // seg * seg
        return {'x': _w((lambda *s: torch.randn(*s, dtype=torch.bfloat16) * 0.05)(B, S, Dm)), 'mem0': _w((lambda *s: torch.randn(*s, dtype=torch.bfloat16) * 1.6)(B, M, Dm)), 'Wq': _w((lambda *s: torch.randn(*s, dtype=torch.bfloat16) * 0.8)(Dm, H, Dh) * Dm ** (-0.5)), 'Wk': _w((lambda *s: torch.randn(*s, dtype=torch.bfloat16) * 0.8)(Dm, H, Dh) * Dm ** (-0.5)), 'Wv': _w((lambda *s: torch.randn(*s, dtype=torch.bfloat16) * 1.6)(Dm, H, Dh) * Dm ** (-0.5)), 'Wo': _w((lambda *s: torch.randn(*s, dtype=torch.bfloat16) * 1.6)(H, Dh, Dm) * (H * Dh) ** (-0.5)), 'seg_len': seg}
    _A_in = _A_setup()

    def _B_setup():
        B, S, D, F = (1, 2048, 512, 1792)
        Fh = F // 2
        return {'x': _w((lambda *s: torch.randn(*s, dtype=torch.bfloat16) * 0.05)(B, S, D)), 'w_in': _w((lambda *s: torch.randn(*s, dtype=torch.bfloat16) * 0.2)(D, 2 * Fh)), 'w_sp': _w(0.01 * (lambda *s: torch.randn(*s, dtype=torch.bfloat16) * 0.2)(S, S)), 'b_sp': _w(1.0 + 0.01 * (lambda *s: torch.randn(*s, dtype=torch.bfloat16) * 0.05)(S)), 'w_out': _w((lambda *s: torch.randn(*s, dtype=torch.bfloat16) * 0.05)(Fh, D))}
    _B_in = _B_setup()
    return {'A_x': _A_in['x'], 'A_mem0': _A_in['mem0'], 'A_Wq': _A_in['Wq'], 'A_Wk': _A_in['Wk'], 'A_Wv': _A_in['Wv'], 'A_Wo': _A_in['Wo'], 'A_seg_len': _A_in['seg_len'], 'B_x': _B_in['x'], 'B_w_in': _B_in['w_in'], 'B_w_sp': _B_in['w_sp'], 'B_b_sp': _B_in['b_sp'], 'B_w_out': _B_in['w_out']}

def reference(A_x, A_mem0, A_Wq, A_Wk, A_Wv, A_Wo, A_seg_len, B_x, B_w_in, B_w_sp, B_b_sp, B_w_out):

    def _A_ref(x, mem0, Wq, Wk, Wv, Wo, seg_len):
        B, S, Dm = x.shape
        H, Dh = (Wq.shape[1], Wq.shape[2])
        M = mem0.shape[1]
        scale = Dh ** (-0.5)
        n_seg = S // seg_len
        L = M + seg_len
        idx = torch.arange(L)
        mask = (idx[:, None] < M) | (idx[:, None] >= idx[None, :])
        mem = mem0
        outs = []
        for s in range(n_seg):
            xs = x[:, s * seg_len:(s + 1) * seg_len, :].clone()
            h = torch.concatenate([mem, xs], axis=1)
            q = torch.einsum('btd,dhe->bhte', h, Wq)
            k = torch.einsum('btd,dhe->bhte', h, Wk)
            v = torch.einsum('btd,dhe->bhte', h, Wv)
            scores = torch.einsum('bhqe,bhke->bhqk', q, k) * scale
            scores = torch.where(mask[None, None], scores, -1000000000.0)
            p = _softmax(scores, axis=-1)
            o = torch.einsum('bhqk,bhke->bhqe', p, v)
            o = torch.einsum('bhte,hed->btd', o, Wo)
            mem = o[:, :M, :].clone()
            outs.append(o[:, M:, :])
        return torch.concatenate(outs, axis=1)

    def _B_ref(x, w_in, w_sp, b_sp, w_out):
        B, S, D = x.shape
        F = w_in.shape[-1]
        Fh = F // 2
        z = _gelu(torch.einsum('bsd,df->bsf', x, w_in))
        z1 = z[..., :Fh].clone()
        z2 = _layernorm(z[..., Fh:], axis=-1)
        g = torch.einsum('ts,bsf->btf', w_sp, z2) + b_sp[None, :, None]
        return torch.einsum('bsf,fd->bsd', z1 * g, w_out)
    x = A_x
    xn = _layernorm(x, axis=-1)
    return x + _A_ref(xn, A_mem0, A_Wq, A_Wk, A_Wv, A_Wo, A_seg_len) + _B_ref(xn, B_w_in, B_w_sp, B_b_sp, B_w_out)
```

```cpp
#include <hip/hip_runtime.h>
#include <stdint.h>
#include <stdlib.h>

typedef __bf16 bf16x2 __attribute__((ext_vector_type(2)));
typedef __bf16 bf16x4 __attribute__((ext_vector_type(4)));
typedef __bf16 bf16x8 __attribute__((ext_vector_type(8)));
typedef __bf16 bf16x16 __attribute__((ext_vector_type(16)));
typedef float f32x4 __attribute__((ext_vector_type(4)));
typedef float f32x2 __attribute__((ext_vector_type(2)));
typedef unsigned u32x4 __attribute__((ext_vector_type(4)));
typedef unsigned u32x2 __attribute__((ext_vector_type(2)));
typedef unsigned short u16;
#define LAS __attribute__((address_space(3)))
#define GAS __attribute__((address_space(1)))
typedef __amdgpu_buffer_rsrc_t rsrc_t;

constexpr int S_ = 2048, DM = 512, NH = 32, DH = 128, NSEG = 64, FH = 896;
constexpr size_t OFF_X = 0;
constexpr size_t X_M = 1u << 20;
constexpr size_t X_TS = X_M + 65536;
constexpr size_t OFF_FLG = 2u << 20;
constexpr size_t OFF_XN = 4u << 20;
constexpr size_t OFF_KF = 8u << 20;
constexpr size_t OFF_VF = 24u << 20;
constexpr size_t OFF_QF = 40u << 20;
constexpr size_t OFF_KM = 56u << 20;
constexpr size_t OFF_VM = 60u << 20;
constexpr size_t OFF_Z1 = 64u << 20;
constexpr size_t OFF_Z2 = 68u << 20;
constexpr size_t OFF_ZN = 72u << 20;
constexpr size_t OFF_SL = 128u << 20;
constexpr int NSLAB = 39;
constexpr int FLG_SEL = 0;
constexpr int FLG_BYTES = 65536;
constexpr int F_KV = 2, F_Q = 34, F_Z2 = 50, F_Z1 = 64, F_CH = 72, F_AH = 136, F_LN = 168, F_SP = 232;
constexpr int SPIN_LIMIT = 200000;

__device__ __forceinline__ rsrc_t mkrsrc(const void* p, unsigned bytes) { return __builtin_amdgcn_make_buffer_rsrc((void*)p, 0, bytes, 0x00020000); }
__device__ __forceinline__ u32x4 ld128(rsrc_t r, unsigned v, unsigned s, int aux) {
  return aux ? __builtin_amdgcn_raw_buffer_load_b128(r, v, s, 16) : __builtin_amdgcn_raw_buffer_load_b128(r, v, s, 0);
}
__device__ __forceinline__ void st128(u32x4 v, rsrc_t r, unsigned vo, unsigned so) {
  __builtin_amdgcn_raw_buffer_store_b128(v, r, vo, so, 16);
  asm volatile("s_nop 1" :: "v"(v));
}
__device__ __forceinline__ unsigned pk2(float a, float b) {
  bf16x2 v = {(__bf16)a, (__bf16)b};
  return __builtin_bit_cast(unsigned, v);
}
__device__ __forceinline__ float bflo(unsigned u) { return __builtin_bit_cast(float, u << 16); }
__device__ __forceinline__ float bfhi(unsigned u) { return __builtin_bit_cast(float, u & 0xffff0000u); }
__device__ __forceinline__ void lds_barrier() { asm volatile("s_waitcnt lgkmcnt(0)\n\ts_barrier" ::: "memory"); }
__device__ __forceinline__ bf16x4 trrd(LAS char* p) { return __builtin_amdgcn_ds_read_tr16_b64_v4bf16((LAS bf16x4*)p); }
__device__ __forceinline__ bf16x8 cat8(bf16x4 a, bf16x4 b) { return __builtin_shufflevector(a, b, 0, 1, 2, 3, 4, 5, 6, 7); }
__device__ __forceinline__ bf16x16 cat16(bf16x8 a, bf16x8 b) { return __builtin_shufflevector(a, b, 0, 1, 2, 3, 4, 5, 6, 7, 8, 9, 10, 11, 12, 13, 14, 15); }
__device__ __forceinline__ bf16x8 asb8(u32x4 v) { return __builtin_bit_cast(bf16x8, v); }
__device__ __forceinline__ f32x4 mfma16(bf16x8 a, bf16x8 b, f32x4 c) { return __builtin_amdgcn_mfma_f32_16x16x32_bf16(a, b, c, 0, 0, 0); }
__device__ __forceinline__ f32x4 smfma(bf16x8 a, bf16x16 b, f32x4 c, int idx) { return __builtin_amdgcn_smfmac_f32_16x16x64_bf16(a, b, c, idx, 0, 0); }
__device__ __forceinline__ void glds16(const void* g, unsigned ldsoff) {
  unsigned keep;
  asm volatile("s_mov_b32 %0, m0\n\ts_mov_b32 m0, %2\n\ts_nop 0\n\tglobal_load_lds_dwordx4 %1, off\n\ts_mov_b32 m0, %0" : "=&s"(keep) : "v"(g), "s"(ldsoff) : "memory");
}
#define SMF_A(acc, a, b) asm volatile("v_smfmac_f32_16x16x64_bf16 %0, %1, %2, %3" : "+v"(acc) : "v"(a), "a"(b), "v"(sidx))
#define SMF_V(acc, a, b) asm volatile("v_smfmac_f32_16x16x64_bf16 %0, %1, %2, %3" : "+v"(acc) : "v"(a), "v"(b), "v"(sidx))
#define MF(acc, a, b) asm volatile("v_mfma_f32_16x16x32_bf16 %0, %1, %2, %0" : "+v"(acc) : "v"(a), "v"(b))
#define MF0(acc, a, b) asm volatile("v_mfma_f32_16x16x32_bf16 %0, %1, %2, 0" : "=&v"(acc) : "v"(a), "v"(b))
#define MFN(acc, a, b) asm volatile("s_nop 1\n\tv_mfma_f32_16x16x32_bf16 %0, %1, %2, %0" : "+v"(acc) : "v"(a), "v"(b))
#define MFN0(acc, a, b) asm volatile("s_nop 1\n\tv_mfma_f32_16x16x32_bf16 %0, %1, %2, 0" : "=&v"(acc) : "v"(a), "v"(b))
#define MFm(acc, a, b) asm volatile("v_mfma_f32_16x16x32_bf16 %0, %1, %2, %0" : "+v"(acc) : "v"(a), "v"(b) : "memory")
#define MF0m(acc, a, b) asm volatile("v_mfma_f32_16x16x32_bf16 %0, %1, %2, 0" : "=&v"(acc) : "v"(a), "v"(b) : "memory")
__device__ __forceinline__ f32x4 zero4() {
  f32x2 lo, hi;
  asm volatile("v_mov_b64 %0, 0\n\tv_mov_b64 %1, 0" : "=v"(lo), "=v"(hi));
  return __builtin_shufflevector(lo, hi, 0, 1, 2, 3);
}
#define PIN1(n, x) asm volatile("s_nop " #n : "+v"(x))
#define PIN2(n, x, y) asm volatile("s_nop " #n : "+v"(x), "+v"(y))
#define PIN4(n, x, y, z, w) asm volatile("s_nop " #n : "+v"(x), "+v"(y), "+v"(z), "+v"(w))
__device__ __forceinline__ unsigned clk32() { unsigned long long t; asm volatile("s_memtime %0\n\ts_waitcnt lgkmcnt(0)" : "=s"(t) :: "memory"); return (unsigned)t; }
__device__ __forceinline__ void spin_delay(int n) {
  asm volatile("s_cmp_lt_i32 %0, 1\n\ts_cbranch_scc1 2f\n1:\n\ts_sub_i32 %0, %0, 1\n\ts_cmp_gt_i32 %0, 0\n\ts_cbranch_scc1 1b\n2:" : "+s"(n) :: "scc", "memory");
}
__device__ __forceinline__ float max3f(float a, float b, float c) { float r; asm("v_max3_f32 %0, %1, %2, %3" : "=v"(r) : "v"(a), "v"(b), "v"(c)); return r; }
__device__ __forceinline__ float max2f(float a, float b) { float r; asm("v_max_f32 %0, %1, %2" : "=v"(r) : "v"(a), "v"(b)); return r; }
__device__ __forceinline__ void swap32(float x, float& r0, float& r1) {
  asm("v_mov_b32 %0, %2\n\tv_mov_b32 %1, %2\n\ts_nop 1\n\tv_permlane32_swap_b32 %0, %1" : "=&v"(r0), "=&v"(r1) : "v"(x));
}
__device__ __forceinline__ void swap16(float x, float& r0, float& r1) {
  asm("v_mov_b32 %0, %2\n\tv_mov_b32 %1, %2\n\ts_nop 1\n\tv_permlane16_swap_b32 %0, %1" : "=&v"(r0), "=&v"(r1) : "v"(x));
}
__device__ __forceinline__ float xmax_g(float x) {
  float t;
  asm("v_mov_b32 %1, %0\n\ts_nop 1\n\tv_permlane32_swap_b32 %0, %1\n\tv_max_f32 %0, %0, %1\n\tv_mov_b32 %1, %0\n\ts_nop 1\n\tv_permlane16_swap_b32 %0, %1\n\tv_max_f32 %0, %0, %1"
      : "+v"(x), "=&v"(t));
  return x;
}
__device__ __forceinline__ float xsum_g(float x) {
  float t;
  asm("v_mov_b32 %1, %0\n\ts_nop 1\n\tv_permlane32_swap_b32 %0, %1\n\tv_add_f32 %0, %0, %1\n\tv_mov_b32 %1, %0\n\ts_nop 1\n\tv_permlane16_swap_b32 %0, %1\n\tv_add_f32 %0, %0, %1"
      : "+v"(x), "=&v"(t));
  return x;
}

struct Params {
  const u16* x; const u16* mem0; const u16* Wq; const u16* Wk; const u16* Wv; const u16* Wo;
  const u16* w_in; const u16* w_sp; const u16* b_sp; const u16* w_out;
  u16* out; char* ws;
};

__global__ void __launch_bounds__(256) k0_prep(Params p) {
  const int tid = threadIdx.x, lane = tid & 63, wave = tid >> 6, b = blockIdx.x;
  char* ws = p.ws;
  if (b < 256) {
#pragma unroll
    for (int rr = 0; rr < 2; ++rr) {
      const int row = b * 8 + wave * 2 + rr;
      const u32x4 v = *(const u32x4*)(p.x + (size_t)row * DM + lane * 8);
      float f[8] = {bflo(v.x), bfhi(v.x), bflo(v.y), bfhi(v.y), bflo(v.z), bfhi(v.z), bflo(v.w), bfhi(v.w)};
      float s = 0.f;
#pragma unroll
      for (int j = 0; j < 8; ++j) s += f[j];
#pragma unroll
      for (int m = 1; m < 64; m <<= 1) s += __shfl_xor(s, m);
      const float mean = s * (1.f / DM);
      float q = 0.f;
#pragma unroll
      for (int j = 0; j < 8; ++j) { f[j] -= mean; q += f[j] * f[j]; }
#pragma unroll
      for (int m = 1; m < 64; m <<= 1) q += __shfl_xor(q, m);
      const float rs = 1.f / sqrtf(q * (1.f / DM) + 1e-5f);
      u32x4 o = {pk2(f[0] * rs, f[1] * rs), pk2(f[2] * rs, f[3] * rs), pk2(f[4] * rs, f[5] * rs), pk2(f[6] * rs, f[7] * rs)};
      const int seg = row >> 5, tau = row & 31, T = (tau >> 2) & 1, i = 4 * (tau >> 3) + (tau & 3);
      *(u32x4*)(ws + OFF_XN + ((size_t)(seg * 2 + T) * 16 + (lane >> 2)) * 1024 + ((lane & 3) * 16 + i) * 16) = o;
    }
    *(u32x4*)(ws + OFF_X + (size_t)b * 4096 + tid * 16) = (u32x4){0, 0, 0, 0};
    if (b < 28) *(u32x4*)(ws + OFF_X + X_M + 16384 + (size_t)b * 4096 + tid * 16) = (u32x4){0, 0, 0, 0};
    if (b >= 32 && b < 48) *(u32x4*)(ws + OFF_FLG + 256 + (size_t)(b - 32) * 4080 + (tid < 255 ? tid : 254) * 16) = (u32x4){0, 0, 0, 0};
  } else if (b == 256) {
    for (int d = tid; d < 2048; d += 256) {
      const int line = d >> 5, rp = (d >> 4) & 1, cc = (d >> 1) & 7, t0 = (d & 1) * 2;
      const int bb = line & 1, g = (line >> 1) & 3, s = line >> 3;
      const int r = 2 * g + rp, k0 = 64 * s + 16 * t0 + 8 * bb + cc;
      unsigned v = (unsigned)p.mem0[r * DM + k0] | ((unsigned)p.mem0[r * DM + k0 + 16] << 16);
      v = v > 1u ? v : 1u;
      *(unsigned*)(ws + OFF_X + X_M + d * 4) = v;
      *(unsigned*)(ws + OFF_X + X_M + 8192 + d * 4) = v;
    }
  } else {
    if (tid == 0) {
      const char* base = ws + OFF_X + X_M + 0x20000;
      unsigned best[2] = {~0u, ~0u}; unsigned sink = 0;
      asm volatile("global_load_dword %0, %1, off sc1\n\ts_waitcnt vmcnt(0)" : "=&v"(sink) : "v"(base + 0x10000) : "memory");
      for (int r = 0; r < 4; ++r)
        for (int par = 0; par < 2; ++par) {
          const char* a = base + r * 16384 + par * 8192 + 1024; unsigned x;
          unsigned t0 = clk32();
          asm volatile("global_load_dword %0, %1, off sc1\n\ts_waitcnt vmcnt(0)" : "=&v"(x) : "v"(a) : "memory");
          unsigned t1 = clk32();
          sink += x & 0;
          best[par] = min(best[par], t1 - t0 + (x & 0));
        }
      const unsigned xcc = (unsigned)__builtin_amdgcn_s_getreg(20 | (3 << 11)) & 0xfu;
      unsigned sel = (best[1] < best[0]) ? 1u : 0u;
      sel ^= (xcc >> 2) & 1u;
      unsigned* f = (unsigned*)(ws + OFF_FLG);
      f[FLG_SEL] = sel; f[1] = best[0]; f[2] = best[1]; f[3] = xcc;
    }
  }
}

__device__ __forceinline__ float gelu_tanh(float x) {
  const float u = 0.7978845608028654f * (x + 0.044715f * x * x * x);
  const float e = __builtin_amdgcn_exp2f(u * 2.885390081777927f);
  const float th = 1.f - 2.f * __builtin_amdgcn_rcpf(e + 1.f);
  return 0.5f * x * (1.f + th);
}

__device__ __forceinline__ void wait_flags(rsrc_t rF, unsigned off, unsigned want, int& budget) {
  const bool act = off != 0x80000000u;
  for (int spin = 0; spin < budget; ++spin) {
    const unsigned v = __builtin_amdgcn_raw_buffer_load_b32(rF, off, 0, 16);
    if (__builtin_amdgcn_ballot_w64(act && v < want) == 0) return;
    __builtin_amdgcn_s_sleep(2);
    asm volatile("" ::: "memory");
  }
  budget = 0;
}
__device__ __forceinline__ void put_flag(rsrc_t rF, unsigned off, unsigned v, int lane) {
  __builtin_amdgcn_raw_buffer_store_b32(v, rF, lane == 0 ? off : 0x80000000u, 0, 16);
}
template <int N> __device__ __forceinline__ void vmwait() { asm volatile("s_waitcnt vmcnt(%0)" :: "i"(N) : "memory"); }

template <int KIND, int NT>
__device__ __forceinline__ void proj_role(const Params& p, LAS char* lds, int j) {
  const int tid = threadIdx.x, lane = tid & 63, wave = __builtin_amdgcn_readfirstlane(tid >> 6);
  const int g = lane >> 4, c = lane & 15;
  char* ws = p.ws;
  const unsigned lbase = (unsigned)(uintptr_t)lds;
  constexpr int NG = 4 * NT, RB = 128 * NT, LPR = 8 * NT, RPI = 8 / NT;
  const char* xn = ws + OFF_XN;
  bf16x8 w[NT][16];
  const int ldw = (KIND >= 2) ? 2 * FH : NH * DH;
  auto rotf = [](int row) { return NT == 1 ? ((row >> 1) & 1) + 2 * ((row >> 3) & 1) : (row & 3) + 4 * ((row >> 3) & 1); };
#pragma unroll
  for (int half = 0; half < 2; ++half) {
    if (half) lds_barrier();
#pragma unroll 4
    for (int n = 0; n < 8 * NT; ++n) {
      const int row = (wave * 8 * NT + n) * RPI + lane / LPR;
      const int x = lane % LPR;
      const int G = ((x >> 1) - rotf(row)) & (NG - 1);
      const int k = half * 256 + row;
      const u16* src;
      if (KIND == 0) src = (G < 8 ? p.Wk : p.Wv) + (size_t)k * ldw + j * 128 + (G & 7) * 16 + (x & 1) * 8;
      else if (KIND == 1) src = p.Wq + (size_t)k * ldw + j * 256 + G * 16 + (x & 1) * 8;
      else if (KIND == 2) src = p.w_in + (size_t)k * ldw + j * 128 + G * 16 + (x & 1) * 8;
      else src = p.w_in + (size_t)k * ldw + FH + j * 64 + G * 16 + (x & 1) * 8;
      glds16(src, lbase + (wave * 8 * NT + n) * 1024);
    }
    vmwait<0>();
    lds_barrier();
#pragma unroll
    for (int ks = 0; ks < 8; ++ks)
#pragma unroll
      for (int t = 0; t < NT; ++t) {
        bf16x4 pc[2];
#pragma unroll
        for (int u = 0; u < 2; ++u) {
          const int row = 32 * ks + 8 * g + 4 * u + (c >> 2);
          pc[u] = trrd(lds + row * RB + ((wave * NT + t + rotf(row)) & (NG - 1)) * 32 + (c & 3) * 8);
        }
        w[t][half * 8 + ks] = cat8(pc[0], pc[1]);
      }
  }
  lds_barrier();
  auto stage = [&](int s) {
    const unsigned dst = lbase + (s % 3) * 32768;
#pragma unroll
    for (int n = 0; n < 8; ++n) glds16(xn + (size_t)s * 32768 + (wave * 8 + n) * 1024 + lane * 16, dst + (wave * 8 + n) * 1024);
  };
  stage(0); stage(1);
  vmwait<8>();
  const rsrc_t rF = mkrsrc(ws + OFF_FLG, FLG_BYTES);
  const unsigned foff = (KIND == 0 ? F_KV : KIND == 1 ? F_Q : KIND == 2 ? F_Z1 : F_Z2) * 128 + j * 128 + wave * 4;
  const bool vform = (KIND == 0 && wave >= 2) || KIND == 3;
  for (int s = 0; s < NSEG; ++s) {
    lds_barrier();
    if (s + 2 < NSEG) stage(s + 2);
    LAS char* src = lds + (s % 3) * 32768 + lane * 16;
    f32x4 acc[2][NT] = {};
#pragma unroll
    for (int hk = 0; hk < 2; ++hk) {
      if (vform) {
#pragma unroll
        for (int ks = hk * 8; ks < hk * 8 + 8; ++ks)
#pragma unroll
          for (int T = 0; T < 2; ++T) {
            const bf16x8 xf = *(LAS bf16x8*)(src + (T * 16 + ks) * 1024);
#pragma unroll
            for (int t = 0; t < NT; ++t) acc[T][t] = mfma16(xf, w[t][ks], acc[T][t]);
          }
      } else {
#pragma unroll
        for (int ks = hk * 8; ks < hk * 8 + 8; ++ks)
#pragma unroll
          for (int T = 0; T < 2; ++T) {
            const bf16x8 xf = *(LAS bf16x8*)(src + (T * 16 + ks) * 1024);
#pragma unroll
            for (int t = 0; t < NT; ++t) acc[T][t] = mfma16(w[t][ks], xf, acc[T][t]);
          }
      }
      if (hk == (NT == 4 ? 0 : 1)) {
        if (s + 2 < NSEG) vmwait<8>(); else vmwait<0>();
        if (s) put_flag(rF, foff, s, lane);
      }
    }
    if (KIND >= 2) {
#pragma unroll
      for (int T = 0; T < 2; ++T)
#pragma unroll
        for (int t = 0; t < NT; ++t)
#pragma unroll
          for (int r = 0; r < 4; ++r) acc[T][t][r] = gelu_tanh(acc[T][t][r]);
    }
    u32x4 o[NT]; char* dst[NT];
    if (vform) {
#pragma unroll
      for (int t = 0; t < NT; ++t) {
        o[t] = (u32x4){pk2(acc[0][t][0], acc[0][t][1]), pk2(acc[0][t][2], acc[0][t][3]), pk2(acc[1][t][0], acc[1][t][1]), pk2(acc[1][t][2], acc[1][t][3])};
        if (KIND == 0) dst[t] = ws + OFF_VF + ((size_t)(j * NSEG + s) * 8 + (wave - 2) * 4 + t) * 1024;
        else dst[t] = ws + OFF_Z2 + ((size_t)s * 56 + j * 4 + wave) * 1024;
      }
    } else {
#pragma unroll
      for (int m = 0; m < NT / 2; ++m)
#pragma unroll
        for (int T = 0; T < 2; ++T) {
          const f32x4 a0 = acc[T][2 * m], a1 = acc[T][(2 * m + 1) % NT];
          o[m * 2 + T] = (u32x4){pk2(a0[0], a1[0]), pk2(a0[1], a1[1]), pk2(a0[2], a1[2]), pk2(a0[3], a1[3])};
          if (KIND == 0) dst[m * 2 + T] = ws + OFF_KF + ((size_t)(j * NSEG + s) * 8 + (wave * 2 + m) * 2 + T) * 1024;
          else if (KIND == 1) dst[m * 2 + T] = ws + OFF_QF + ((size_t)((2 * j + (wave >> 1)) * NSEG + s) * 8 + ((wave & 1) * 2 + m) * 2 + T) * 1024;
          else dst[m * 2 + T] = ws + OFF_Z1 + ((size_t)(s * 2 + T) * 28 + (j * 4 + wave)) * 1024;
        }
    }
#pragma unroll
    for (int t = 0; t < NT; ++t)
      asm volatile("global_store_dwordx4 %0, %1, off sc1\n\ts_nop 1" :: "v"(dst[t] + lane * 16), "v"(o[t]) : "memory");
  }
  vmwait<0>();
  put_flag(rF, foff, NSEG, lane);
}

__device__ __forceinline__ float xmax_g(float x);
__device__ __forceinline__ float xsum_g(float x);
__device__ __forceinline__ void ah_role(const Params& p, LAS char* lds, int h) {
  const int tid = threadIdx.x, lane = tid & 63, wave = __builtin_amdgcn_readfirstlane(tid >> 6);
  const int g = lane >> 4, c = lane & 15;
  char* ws = p.ws;
  const unsigned lbase = (unsigned)(uintptr_t)lds;
#pragma unroll 4
  for (int n = 0; n < 32; ++n) {
    const int e = wave * 32 + n;
    const int G = ((lane >> 2) - (e & 3)) & 15;
    glds16(p.Wo + (size_t)(h * DH + e) * DM + G * 32 + (lane & 3) * 8, lbase + e * 1024);
  }
  vmwait<0>();
  lds_barrier();
  bf16x8 wo[4][2][4];
#pragma unroll
  for (int P = 0; P < 4; ++P)
#pragma unroll
    for (int t = 0; t < 2; ++t)
#pragma unroll
      for (int m = 0; m < 4; ++m) {
        bf16x4 pc[2];
#pragma unroll
        for (int u = 0; u < 2; ++u) {
          const int e = 32 * m + 16 * u + 4 * g + (c >> 2);
          pc[u] = trrd(lds + e * 1024 + ((wave * 4 + P + (e & 3)) & 15) * 64 + (c & 3) * 16 + t * 8);
        }
        wo[P][t][m] = cat8(pc[0], pc[1]);
      }
  lds_barrier();
  const rsrc_t rF = mkrsrc(ws + OFF_FLG, FLG_BYTES);
  const rsrc_t rK = mkrsrc(ws + OFF_KF + (size_t)h * NSEG * 8192, NSEG * 8192);
  const rsrc_t rV = mkrsrc(ws + OFF_VF + (size_t)h * NSEG * 8192, NSEG * 8192);
  const rsrc_t rQ = mkrsrc(ws + OFF_QF + (size_t)h * NSEG * 8192, NSEG * 8192);
  const rsrc_t rKM = mkrsrc(ws + OFF_KM + (size_t)h * NSEG * 2048, NSEG * 2048);
  const rsrc_t rVM = mkrsrc(ws + OFF_VM + (size_t)h * NSEG * 2048, NSEG * 2048);
  const rsrc_t rS = mkrsrc(ws + OFF_SL, NSEG * NSLAB * 32768);
  const int T = wave & 1, eh = wave >> 1;
  const unsigned fA = lane < 4 ? (unsigned)((F_KV + h) * 128 + lane * 4) : lane < 6 ? (unsigned)((F_Q + (h >> 1)) * 128 + ((h & 1) * 2 + lane - 4) * 4) : 0x80000000u;
  const unsigned fB = lane < 2 ? (unsigned)((F_CH + 2 * h + lane) * 128) : 0x80000000u;
  const int tq = 8 * (c >> 2) + 4 * T + (c & 3);
  const float CEXP = 0.08838834764831845f * 1.4426950408889634f;
  int budget = SPIN_LIMIT;
  u32x4 kx[8], qx[4], vx[4];
  auto prefetch = [&](int s) {
    wait_flags(rF, fA, s + 1, budget);
#pragma unroll
    for (int n = 0; n < 8; ++n) kx[n] = ld128(rK, lane * 16, s * 8192 + n * 1024, 16);
#pragma unroll
    for (int n = 0; n < 4; ++n) qx[n] = ld128(rQ, lane * 16, s * 8192 + (n * 2 + T) * 1024, 16);
#pragma unroll
    for (int n = 0; n < 4; ++n) vx[n] = ld128(rV, lane * 16, s * 8192 + (eh * 4 + n) * 1024, 16);
  };
  prefetch(0);
  for (int s = 0; s < NSEG; ++s) {
    f32x4 sx[2] = {}, sm = {};
#pragma unroll
    for (int k4 = 0; k4 < 4; ++k4) { sx[0] = mfma16(asb8(kx[k4 * 2]), asb8(qx[k4]), sx[0]); sx[1] = mfma16(asb8(kx[k4 * 2 + 1]), asb8(qx[k4]), sx[1]); }
    wait_flags(rF, fB, s + 1, budget);
    u32x4 km[4]; unsigned vm[4];
#pragma unroll
    for (int k4 = 0; k4 < 4; ++k4) km[k4] = ld128(rKM, g * 128 + (2 * (c >> 2) + (c & 1)) * 16, s * 2048 + k4 * 512, 16);
#pragma unroll
    for (int n = 0; n < 4; ++n) vm[n] = __builtin_amdgcn_raw_buffer_load_b32(rVM, lane * 4, s * 2048 + (eh * 4 + n) * 256, 16);
    float v[10];
#pragma unroll
    for (int T2 = 0; T2 < 2; ++T2)
#pragma unroll
      for (int r = 0; r < 4; ++r) v[T2 * 4 + r] = (8 * g + 4 * T2 + r <= tq) ? sx[T2][r] : -3.0e38f;
#pragma unroll
    for (int k4 = 0; k4 < 4; ++k4) sm = mfma16(asb8(km[k4]), asb8(qx[k4]), sm);
    v[8] = sm[0]; v[9] = sm[1];
    float mm = v[0];
#pragma unroll
    for (int r = 1; r < 10; ++r) mm = fmaxf(mm, v[r]);
    mm = xmax_g(mm);
    float den = 0.f;
#pragma unroll
    for (int r = 0; r < 10; ++r) { v[r] = __builtin_amdgcn_exp2f((v[r] - mm) * CEXP); den += v[r]; }
    const float rinv = 1.f / xsum_g(den);
    const bf16x8 pb = asb8((u32x4){pk2(v[0] * rinv, v[1] * rinv), pk2(v[2] * rinv, v[3] * rinv), pk2(v[4] * rinv, v[5] * rinv), pk2(v[6] * rinv, v[7] * rinv)});
    const bf16x8 pm = asb8((u32x4){pk2(v[8] * rinv, v[9] * rinv), 0, 0, 0});
    f32x4 o[4];
#pragma unroll
    for (int n = 0; n < 4; ++n) { o[n] = mfma16(asb8(vx[n]), pb, (f32x4){}); o[n] = mfma16(asb8((u32x4){vm[n], 0, 0, 0}), pm, o[n]); }
    LAS char* ob = lds + (s & 1) * 8192;
#pragma unroll
    for (int m2 = 0; m2 < 2; ++m2)
      *(LAS u32x4*)(ob + (T * 4 + eh * 2 + m2) * 1024 + lane * 16) =
          (u32x4){pk2(o[2 * m2][0], o[2 * m2][1]), pk2(o[2 * m2][2], o[2 * m2][3]), pk2(o[2 * m2 + 1][0], o[2 * m2 + 1][1]), pk2(o[2 * m2 + 1][2], o[2 * m2 + 1][3])};
    lds_barrier();
    bf16x8 of[8];
#pragma unroll
    for (int n = 0; n < 8; ++n) of[n] = *(LAS bf16x8*)(ob + n * 1024 + lane * 16);
#pragma unroll
    for (int T2 = 0; T2 < 2; ++T2)
#pragma unroll
      for (int P = 0; P < 4; ++P) {
        f32x4 a0 = {}, a1 = {};
#pragma unroll
        for (int m = 0; m < 4; ++m) { a0 = mfma16(wo[P][0][m], of[T2 * 4 + m], a0); a1 = mfma16(wo[P][1][m], of[T2 * 4 + m], a1); }
        st128((u32x4){pk2(a0[0], a0[1]), pk2(a0[2], a0[3]), pk2(a1[0], a1[1]), pk2(a1[2], a1[3])}, rS, lane * 16, (s * NSLAB + h) * 32768 + ((T2 * 4 + wave) * 4 + P) * 1024);
      }
    vmwait<0>();
    put_flag(rF, (F_AH + h) * 128 + wave * 4, s + 1, lane);
    if (s + 1 < NSEG) prefetch(s + 1);
  }
}

__device__ __forceinline__ void red_role(const Params& p, LAS char* lds, int r) {
  const int tid = threadIdx.x, lane = tid & 63, wave = __builtin_amdgcn_readfirstlane(tid >> 6);
  const int g = lane >> 4, c = lane & 15;
  char* ws = p.ws;
  const rsrc_t rF = mkrsrc(ws + OFF_FLG, FLG_BYTES);
  const rsrc_t rS = mkrsrc(ws + OFF_SL, NSEG * NSLAB * 32768);
  const int T = r >> 4, w = (r >> 2) & 3, P = r & 3;
  bf16x8 a0, a1;
#pragma unroll
  for (int j = 0; j < 8; ++j) { a0[j] = (__bf16)((8 * g + j == c) ? 1.f : 0.f); a1[j] = (__bf16)((8 * g + j == 16 + c) ? 1.f : 0.f); }
  const unsigned fA = lane < 8 ? (unsigned)((F_AH + wave * 8 + lane) * 128 + w * 4) : 0x80000000u;
  const int tok = 8 * (c >> 2) + 4 * T + (c & 3);
  int budget = SPIN_LIMIT;
  for (int s = 0; s < NSEG; ++s) {
    f32x4 d0 = {}, d1 = {};
    if ((s & 1) == 0) {
      const unsigned fY = (lane < 2 && wave + 4 * lane < 7) ? (unsigned)((F_SP + (s >> 3) * 7 + wave + 4 * lane) * 128 + ((s >> 1) & 3) * 4) : 0x80000000u;
      wait_flags(rF, fY, 1, budget);
    }
    const unsigned sb = s * NSLAB * 32768 + r * 1024;
    const u32x4 y0 = ld128(rS, lane * 16, sb + (32 + wave) * 32768, 16);
    const u32x4 y1 = ld128(rS, wave < 3 ? (unsigned)(lane * 16) : 0x80000000u, sb + (36 + (wave < 3 ? wave : 0)) * 32768, 16);
    u32x2 xv = {0, 0};
    if (wave < 2) xv = *(const u32x2*)(p.x + (size_t)(s * 32 + tok) * DM + 128 * w + 32 * P + 16 * wave + 4 * g);
    wait_flags(rF, fA, s + 1, budget);
    u32x4 v[8];
#pragma unroll
    for (int n = 0; n < 8; ++n) v[n] = ld128(rS, lane * 16, sb + (wave * 8 + n) * 32768, 16);
    d0 = mfma16(a0, asb8(y0), d0); d1 = mfma16(a1, asb8(y0), d1);
    d0 = mfma16(a0, asb8(y1), d0); d1 = mfma16(a1, asb8(y1), d1);
#pragma unroll
    for (int n = 0; n < 8; ++n) { d0 = mfma16(a0, asb8(v[n]), d0); d1 = mfma16(a1, asb8(v[n]), d1); }
    LAS char* b = lds + (s & 1) * 8192;
    *(LAS f32x4*)(b + (wave * 2) * 1024 + lane * 16) = d0;
    *(LAS f32x4*)(b + (wave * 2 + 1) * 1024 + lane * 16) = d1;
    lds_barrier();
    if (wave < 2) {
      f32x4 t = *(LAS f32x4*)(b + wave * 1024 + lane * 16);
#pragma unroll
      for (int n = 1; n < 4; ++n) t += *(LAS f32x4*)(b + (n * 2 + wave) * 1024 + lane * 16);
      *(u32x2*)(p.out + (size_t)(s * 32 + tok) * DM + 128 * w + 32 * P + 16 * wave + 4 * g) =
          (u32x2){pk2(t[0] + bflo(xv.x), t[1] + bfhi(xv.x)), pk2(t[2] + bflo(xv.y), t[3] + bfhi(xv.y))};
    }
  }
}

__device__ __forceinline__ float rowsum16(float x) {
  x += __builtin_bit_cast(float, __builtin_amdgcn_update_dpp(0, __builtin_bit_cast(int, x), 0x128, 0xf, 0xf, false));
  x += __builtin_bit_cast(float, __builtin_amdgcn_update_dpp(0, __builtin_bit_cast(int, x), 0x124, 0xf, 0xf, false));
  x += __builtin_bit_cast(float, __builtin_amdgcn_update_dpp(0, __builtin_bit_cast(int, x), 0x122, 0xf, 0xf, false));
  x += __builtin_bit_cast(float, __builtin_amdgcn_update_dpp(0, __builtin_bit_cast(int, x), 0x121, 0xf, 0xf, false));
  return x;
}
__device__ __forceinline__ void sp_role(const Params& p, LAS char* lds, int tile) {
  const int tid = threadIdx.x, lane = tid & 63, wave = __builtin_amdgcn_readfirstlane(tid >> 6);
  const int g = lane >> 4, c = lane & 15;
  char* ws = p.ws;
  const unsigned lbase = (unsigned)(uintptr_t)lds;
  const rsrc_t rF = mkrsrc(ws + OFF_FLG, FLG_BYTES);
  int budget = SPIN_LIMIT;
  for (int s = tile; s < NSEG; s += 56) {
    const rsrc_t rZ = mkrsrc(ws + OFF_Z2 + (size_t)s * 57344, 57344);
    const rsrc_t rN = mkrsrc(ws + OFF_ZN + (size_t)s * 57344, 57344);
    wait_flags(rF, lane < 14 ? (unsigned)((F_Z2 + ((wave * 14 + lane) >> 2)) * 128 + ((wave * 14 + lane) & 3) * 4) : 0x80000000u, s + 1, budget);
    u32x4 z[14];
#pragma unroll
    for (int n = 0; n < 14; ++n) z[n] = ld128(rZ, lane * 16, (wave * 14 + n) * 1024, 16);
    float f[14][8], sum[8] = {};
#pragma unroll
    for (int n = 0; n < 14; ++n) {
      f[n][0] = bflo(z[n].x); f[n][1] = bfhi(z[n].x); f[n][2] = bflo(z[n].y); f[n][3] = bfhi(z[n].y);
      f[n][4] = bflo(z[n].z); f[n][5] = bfhi(z[n].z); f[n][6] = bflo(z[n].w); f[n][7] = bfhi(z[n].w);
#pragma unroll
      for (int j = 0; j < 8; ++j) sum[j] += f[n][j];
    }
    LAS float* red = (LAS float*)lds;
#pragma unroll
    for (int j = 0; j < 8; ++j) { const float t = rowsum16(sum[j]); if (c == 0) red[wave * 32 + g * 8 + j] = t; }
    lds_barrier();
    float q[8] = {};
#pragma unroll
    for (int j = 0; j < 8; ++j) {
      const float mean = (red[g * 8 + j] + red[32 + g * 8 + j] + red[64 + g * 8 + j] + red[96 + g * 8 + j]) * (1.f / FH);
#pragma unroll
      for (int n = 0; n < 14; ++n) { f[n][j] -= mean; q[j] += f[n][j] * f[n][j]; }
    }
#pragma unroll
    for (int j = 0; j < 8; ++j) { const float t = rowsum16(q[j]); if (c == 0) red[128 + wave * 32 + g * 8 + j] = t; }
    lds_barrier();
    float rs[8];
#pragma unroll
    for (int j = 0; j < 8; ++j)
      rs[j] = 1.f / sqrtf((red[128 + g * 8 + j] + red[160 + g * 8 + j] + red[192 + g * 8 + j] + red[224 + g * 8 + j]) * (1.f / FH) + 1e-5f);
#pragma unroll
    for (int n = 0; n < 14; ++n)
      st128((u32x4){pk2(f[n][0] * rs[0], f[n][1] * rs[1]), pk2(f[n][2] * rs[2], f[n][3] * rs[3]), pk2(f[n][4] * rs[4], f[n][5] * rs[5]), pk2(f[n][6] * rs[6], f[n][7] * rs[7])},
            rN, lane * 16, (wave * 14 + n) * 1024);
    vmwait<0>();
    lds_barrier();
    put_flag(rF, (F_LN + s) * 128, 1, tid);
  }
  const int tp = tile / 7, fc = tile % 7;
  if (wave == 0) {
    wait_flags(rF, (unsigned)((F_LN + lane) * 128), 1, budget);
    __builtin_amdgcn_fence(__ATOMIC_ACQUIRE, "agent");
    vmwait<0>();
  }
  lds_barrier();
  const char* zn = ws + OFF_ZN;
  const u16* brow[4];
#pragma unroll
  for (int Tt = 0; Tt < 4; ++Tt) brow[Tt] = p.w_sp + (size_t)(tp * 256 + wave * 64 + (Tt >> 1) * 32 + 8 * (c >> 2) + 4 * (Tt & 1) + (c & 3)) * S_ + 8 * g;
  auto stage = [&](int kk) {
    const unsigned dst = lbase + (kk % 3) * 49152;
#pragma unroll
    for (int n = 0; n < 4; ++n) { const int f = wave * 4 + n; glds16(zn + ((size_t)(kk * 2 + (f >> 3)) * 56 + fc * 8 + (f & 7)) * 1024 + lane * 16, dst + f * 1024); }
#pragma unroll
    for (int n = 0; n < 8; ++n) glds16(brow[n & 3] + kk * 64 + (n >> 2) * 32, dst + 16384 + (wave * 8 + n) * 1024);
  };
  f32x4 acc[4][8] = {};
  stage(0); stage(1);
  for (int kk = 0; kk < 32; ++kk) {
    if (kk + 1 < 32) vmwait<12>(); else vmwait<0>();
    lds_barrier();
    if (kk + 2 < 32) stage(kk + 2);
    LAS char* b = lds + (kk % 3) * 49152 + lane * 16;
#pragma unroll
    for (int k2 = 0; k2 < 2; ++k2) {
      bf16x8 bf[4];
#pragma unroll
      for (int Tt = 0; Tt < 4; ++Tt) bf[Tt] = *(LAS bf16x8*)(b + 16384 + (wave * 8 + k2 * 4 + Tt) * 1024);
#pragma unroll
      for (int ft = 0; ft < 8; ++ft) {
        const bf16x8 af = *(LAS bf16x8*)(b + (k2 * 8 + ft) * 1024);
#pragma unroll
        for (int Tt = 0; Tt < 4; ++Tt) acc[Tt][ft] = mfma16(af, bf[Tt], acc[Tt][ft]);
      }
    }
  }
  lds_barrier();
#pragma unroll 4
  for (int n = 0; n < 32; ++n) {
    const int e = wave * 32 + n;
    const int G = ((lane >> 2) - (e & 3)) & 15;
    glds16(p.w_out + (size_t)(fc * 128 + e) * DM + G * 32 + (lane & 3) * 8, lbase + e * 1024);
  }
  wait_flags(rF, lane < 4 ? (unsigned)((F_Z1 + fc) * 128 + lane * 4) : 0x80000000u, tp * 8 + 8, budget);
  const rsrc_t rZ1 = mkrsrc(ws + OFF_Z1, NSEG * 2 * 28 * 1024);
  bf16x8 ub[4][4];
#pragma unroll
  for (int Tt = 0; Tt < 4; ++Tt) {
    const int sg = tp * 8 + wave * 2 + (Tt >> 1);
    const float bias = bflo((unsigned)p.b_sp[sg * 32 + 8 * (c >> 2) + 4 * (Tt & 1) + (c & 3)]);
#pragma unroll
    for (int m = 0; m < 4; ++m) {
      const u32x4 z = ld128(rZ1, lane * 16, ((sg * 2 + (Tt & 1)) * 28 + fc * 4 + m) * 1024, 16);
      const f32x4 g0 = acc[Tt][2 * m] + bias, g1 = acc[Tt][2 * m + 1] + bias;
      ub[Tt][m] = asb8((u32x4){pk2(bflo(z.x) * g0[0], bflo(z.y) * g0[1]), pk2(bflo(z.z) * g0[2], bflo(z.w) * g0[3]),
                               pk2(bfhi(z.x) * g1[0], bfhi(z.y) * g1[1]), pk2(bfhi(z.z) * g1[2], bfhi(z.w) * g1[3])});
    }
  }
  vmwait<0>();
  lds_barrier();
  const rsrc_t rS = mkrsrc(ws + OFF_SL, NSEG * NSLAB * 32768);
  for (int P16 = 0; P16 < 16; ++P16) {
    bf16x8 wf[2][4];
#pragma unroll
    for (int t = 0; t < 2; ++t)
#pragma unroll
      for (int m = 0; m < 4; ++m) {
        bf16x4 pc[2];
#pragma unroll
        for (int u = 0; u < 2; ++u) {
          const int e = 32 * m + 16 * u + 4 * g + (c >> 2);
          pc[u] = trrd(lds + e * 1024 + ((P16 + (e & 3)) & 15) * 64 + (c & 3) * 16 + t * 8);
        }
        wf[t][m] = cat8(pc[0], pc[1]);
      }
#pragma unroll
    for (int Tt = 0; Tt < 4; ++Tt) {
      f32x4 a0 = {}, a1 = {};
#pragma unroll
      for (int m = 0; m < 4; ++m) { a0 = mfma16(wf[0][m], ub[Tt][m], a0); a1 = mfma16(wf[1][m], ub[Tt][m], a1); }
      const int sg = tp * 8 + wave * 2 + (Tt >> 1);
      st128((u32x4){pk2(a0[0], a0[1]), pk2(a0[2], a0[3]), pk2(a1[0], a1[1]), pk2(a1[2], a1[3])}, rS, lane * 16, (sg * NSLAB + 32 + fc) * 32768 + ((Tt & 1) * 16 + P16) * 1024);
    }
  }
  vmwait<0>();
  put_flag(rF, (F_SP + tile) * 128 + wave * 4, 1, lane);
}

constexpr int L_MST = 0, L_KX = 8192, L_VX = 16384, L_QI = 24576, L_OI = 30976, L_WO = 32768, L_S1 = 98304, L_WV = 98304;
constexpr int QI_G = 400, QI_W = 4 * QI_G;
#define ST(i)
#define CNT(i)
#define TRS(x)
#define TA 8
#define TB 8
#define D1 0
#define D2 0

__device__ __forceinline__ unsigned min3u(unsigned a, unsigned b, unsigned c) { return min(min(a, b), c); }
__device__ __forceinline__ unsigned min4u(u32x4 a) { return min(min3u(a.x, a.y, a.z), a.w); }

__device__ __forceinline__ void chain_role(const Params& p, LAS char* lds, int h, int a) {
  const int tid = threadIdx.x, lane = tid & 63, wave = __builtin_amdgcn_readfirstlane(tid >> 6);
  const int g = lane >> 4, c = lane & 15;
  char* ws = p.ws;
  const unsigned lbase = (unsigned)(uintptr_t)lds;
  const unsigned sel = __builtin_amdgcn_readfirstlane(((const unsigned*)(ws + OFF_FLG))[FLG_SEL]) & 1u;

  bf16x16 wq[2][8], wk[2][8], wv[2][8];
  {
    auto issue = [&](const u16* W, int half, int soff) {
#pragma unroll 4
      for (int n = 0; n < 16; ++n) {
        const int row = (wave * 16 + n) * 4 + (lane >> 4);
        const int x = lane & 15;
        const int rot = (row & 3) + 4 * ((row >> 4) & 1);
        const int G = ((x >> 1) - rot) & 7;
        glds16(W + (size_t)(half * 256 + row) * (NH * DH) + h * DH + G * 16 + (x & 1) * 8, lbase + soff + (wave * 16 + n) * 1024);
      }
    };
    auto fetch = [&](bf16x16 (&w)[2][8], int half, int soff) {
#pragma unroll
      for (int m = 0; m < 4; ++m) {
        const int s4 = (wave + m) & 3;
#pragma unroll
        for (int t = 0; t < 2; ++t) {
          bf16x4 pc[4];
#pragma unroll
          for (int u = 0; u < 4; ++u) {
            const int q = c >> 2;
            const int row = 64 * s4 + 32 * (u & 1) + 16 * (q & 1) + 8 * (u >> 1) + 4 * (g >> 1) + 2 * (q >> 1) + (g & 1);
            const int rot = (row & 3) + 4 * ((row >> 4) & 1);
            pc[u] = trrd(lds + soff + row * 256 + ((wave * 2 + t + rot) & 7) * 32 + (c & 3) * 8);
          }
          w[t][2 * m + half] = cat16(cat8(pc[0], pc[1]), cat8(pc[2], pc[3]));
        }
      }
    };
    issue(p.Wq, 0, 0); issue(p.Wq, 1, L_S1);
    asm volatile("s_waitcnt vmcnt(16)" ::: "memory"); lds_barrier();
    fetch(wq, 0, 0); lds_barrier();
    issue(p.Wk, 0, 0);
    asm volatile("s_waitcnt vmcnt(16)" ::: "memory"); lds_barrier();
    fetch(wq, 1, L_S1); lds_barrier();
    issue(p.Wk, 1, L_S1);
    asm volatile("s_waitcnt vmcnt(16)" ::: "memory"); lds_barrier();
    fetch(wk, 0, 0); lds_barrier();
    issue(p.Wv, 0, 0);
    asm volatile("s_waitcnt vmcnt(16)" ::: "memory"); lds_barrier();
    fetch(wk, 1, L_S1); lds_barrier();
    issue(p.Wv, 1, L_S1);
    asm volatile("s_waitcnt vmcnt(16)" ::: "memory"); lds_barrier();
    fetch(wv, 0, 0); lds_barrier();
#pragma unroll 4
    for (int n = 0; n < 16; ++n) {
      const int e = (wave * 16 + n) * 2 + (lane >> 5);
      const int x = lane & 31;
      const int rot = (e & 1) + 2 * ((e >> 2) & 1) + 4 * ((e >> 3) & 1);
      const int G = ((x >> 1) - rot) & 15;
      glds16(p.Wo + (size_t)(h * DH + e) * DM + a * 256 + G * 16 + (x & 1) * 8, lbase + L_WO + (wave * 16 + n) * 1024);
    }
    asm volatile("s_waitcnt vmcnt(16)" ::: "memory"); lds_barrier();
    fetch(wv, 1, L_S1);
    asm volatile("s_waitcnt vmcnt(0)" ::: "memory"); lds_barrier();
    {
      bf16x8 wlo[8], whi[8];
#pragma unroll
      for (int n = 0; n < 8; ++n) {
        const int q = c >> 2, rot = (q & 1) + 2 * (g & 1) + 4 * (q >> 1);
        LAS char* b = lds + L_WO + (64 * (n >> 2) + 16 * (g >> 1) + 8 * (q >> 1) + 4 * (g & 1) + (q & 1)) * 512 + ((wave * 4 + (n & 3) + rot) & 15) * 32 + (c & 3) * 8;
        wlo[n] = cat8(trrd(b), trrd(b + 2 * 512)); whi[n] = cat8(trrd(b + 32 * 512), trrd(b + 34 * 512));
      }
      lds_barrier();
#pragma unroll
      for (int n = 0; n < 8; ++n) { LAS char* d = lds + L_WO + (wave * 8 + n) * 2048 + lane * 16; *(LAS bf16x8*)d = wlo[n]; *(LAS bf16x8*)(d + 1024) = whi[n]; }
    }
#pragma unroll
    for (int t = 0; t < 2; ++t)
#pragma unroll
      for (int u = 5; u < 8; ++u) {
        LAS char* d = lds + L_WV + (wave * 6 + (u == 5 ? 4 : (u - 6) * 2) + t) * 2048 + lane * 16;
        *(LAS bf16x8*)d = __builtin_shufflevector(wv[t][u], wv[t][u], 0, 1, 2, 3, 4, 5, 6, 7);
        *(LAS bf16x8*)(d + 1024) = __builtin_shufflevector(wv[t][u], wv[t][u], 8, 9, 10, 11, 12, 13, 14, 15);
      }
#pragma unroll
    for (int t = 0; t < 2; ++t)
#pragma unroll
      for (int u = 0; u < 8; ++u) {
        asm volatile("" : "+a"(wk[t][u]));
        if (u < 5) asm volatile("" : "+a"(wv[t][u])); else asm volatile("" : "+a"(wq[t][u]));
      }
  }

  const rsrc_t rX = mkrsrc(ws + OFF_X, (2u << 20) + FLG_BYTES);
  const rsrc_t rK = mkrsrc(ws + OFF_KF + (size_t)h * NSEG * 8192, NSEG * 8192);
  const rsrc_t rV = mkrsrc(ws + OFF_VF + (size_t)h * NSEG * 8192, NSEG * 8192);
  const rsrc_t rKVM = mkrsrc(ws + (a ? OFF_VM : OFF_KM) + (size_t)h * NSEG * 2048, NSEG * 2048);
  const unsigned kvd = a ? 256u : 16u;
  const int i16 = c, pr = (i16 >> 1) & 1;
  const int sidx = pr ? 0xEEEE : 0x4444;
  const unsigned vM = X_M + sel * 8192 + wave * 1024 + (i16 >> 2) * 256 + (g >> 1) * 128 + (i16 & 1) * 64 + (g & 1) * 32 + pr * 16;
  const int myline = ((a * 4 + wave) * 4 + g) * 2 + (c >> 3);
  const unsigned vPst = (myline >> 1) * 16384 + sel * 8192 + (myline & 1) * 4096 + h * 128 + (c & 7) * 16;
  const int rline = h * 2 + a;
  const unsigned vPld = (rline >> 1) * 16384 + sel * 8192 + (rline & 1) * 4096 + lane * 16;
  const unsigned vMst = ((g < 2) && (c < 8)) ? (unsigned)(X_M + sel * 8192 + rline * 128 + g * 64 + c * 8) : 0x80000000u;
  const bool lo8 = c < 8;
  const float CEXP = 0.08838834764831845f * 1.4426950408889634f;
  int spin_limit = SPIN_LIMIT;
  const unsigned vKV0 = a ? (unsigned)((2 * wave) * 256 + lane * 4) : (unsigned)(wave * 512 + (c >> 2) * 128 + (2 * g) * 16 + (c & 3) * 4);
  int qi_st = L_QI + wave * QI_W + (c >> 2) * QI_G + (2 * g) * 16 + (c & 3) * 4;
  asm volatile("" : "+v"(qi_st));
  int qf_ld = L_QI + g * QI_G + (c & 7) * 16;
  asm volatile("" : "+v"(qf_ld));
  const int kf_ld = L_QI + g * QI_G + 256 + (2 * (c >> 2) + (c & 1)) * 16;
  int wo_ld = L_WO + wave * 16384 + lane * 16, wv_ld = L_WV + wave * 12288 + lane * 16;
  asm volatile("" : "+v"(wo_ld), "+v"(wv_ld));
  const int oi_st = L_MST + (wave >> 1) * 1024 + ((2 * (wave & 1) + (c >> 3)) * 16 + 4 * ((c & 7) >> 1) + 2 * (g >> 1) + (c & 1)) * 16 + (g & 1) * 8;

  u32x4 pf[4];
  auto kv_issue = [&](int s) {
    pf[0] = ld128(rK, lane * 16, s * 8192 + wave * 2048, 16); pf[1] = ld128(rK, lane * 16, s * 8192 + wave * 2048 + 1024, 16);
    pf[2] = ld128(rV, lane * 16, s * 8192 + wave * 2048, 16); pf[3] = ld128(rV, lane * 16, s * 8192 + wave * 2048 + 1024, 16);
  };
  auto kv_commit = [&]() {
    LAS char* d = lds + wave * 2048 + lane * 16;
    *(LAS u32x4*)(d + L_KX) = pf[0]; *(LAS u32x4*)(d + L_KX + 1024) = pf[1]; *(LAS u32x4*)(d + L_VX) = pf[2]; *(LAS u32x4*)(d + L_VX + 1024) = pf[3];
  };
  wait_flags(rX, lane < 4 ? (unsigned)(OFF_FLG + (F_KV + h) * 128 + lane * 4) : 0x80000000u, 2, spin_limit);
  kv_issue(0); kv_commit();
  int mst_st = L_MST + wave * 1024 + lane * 16, vx_ld = L_VX + wave * 2048 + lane * 16;
  asm volatile("" : "+v"(mst_st), "+v"(vx_ld));
  const unsigned vTS = wave == 3 ? (unsigned)(X_TS + sel * 8192 + lane * 128) : 0x80000000u;
  const unsigned vTSst = (lane == 0 && wave == 3) ? (unsigned)(X_TS + sel * 8192 + rline * 128) : 0x80000000u;
  const unsigned vPz = vPld + wave * 1024;
  const unsigned vCH = (tid == 64) ? (unsigned)(OFF_FLG + (F_CH + rline) * 128) : 0x80000000u;
  const unsigned vMz = wave == 3 ? vMst : 0x80000000u;
  if (wave == 3) {
    bf16x8 asel;
#pragma unroll
    for (int j = 0; j < 8; ++j) asel[j] = (__bf16)((c == j) ? 1.0f : 0.0f);
    *(LAS bf16x8*)(lds + L_OI + lane * 16) = asel;
  }
  int mst_ld[3];
#pragma unroll
  for (int m = 1; m < 4; ++m) { mst_ld[m - 1] = L_MST + ((wave + m) & 3) * 1024 + lane * 16; asm volatile("" : "+v"(mst_ld[m - 1])); }

  f32x4 aq0 = zero4(), aq1 = zero4(), ak0 = zero4(), ak1 = zero4(), av0 = zero4(), av1 = zero4();
  lds_barrier();
  bf16x8 kxf[8];
#pragma unroll
  for (int n = 0; n < 8; ++n) kxf[n] = *(LAS bf16x8*)(lds + L_KX + n * 1024 + lane * 16);
  u32x4 f0 = ld128(rX, vM, 0, 16), f1 = ld128(rX, vM, 4096, 16);
#define FENCE() asm volatile("" ::: "memory")
#define KEEP(x) asm volatile("" : "+v"(x))
#define Q0(u) do { if ((u) < 5) SMF_V(aq0, af[u], wq[0][u]); else SMF_A(aq0, af[u], wq[0][u]); } while (0)
#define Q1(u) do { if ((u) < 5) SMF_V(aq1, af[u], wq[1][u]); else SMF_A(aq1, af[u], wq[1][u]); } while (0)
#define QQ(u) do { Q0(u); Q1(u); } while (0)
#define K0(u) SMF_A(ak0, af[u], wk[0][u])
#define K1(u) SMF_A(ak1, af[u], wk[1][u])
#define KK(u) do { K0(u); K1(u); } while (0)
#define V0(u) SMF_A(av0, af[u], wv[0][u])
#define V1(u) SMF_A(av1, af[u], wv[1][u])
#define VV(u) do { V0(u); V1(u); } while (0)
#define LDA(u) do { FENCE(); af[u] = *(LAS bf16x8*)(lds + mst_ld[((u) >> 1) - 1] + ((u) & 1) * 4096); FENCE(); } while (0)
#define LDF(base, n) cat16(*(LAS bf16x8*)(lds + (base) + (n) * 2048), *(LAS bf16x8*)(lds + (base) + (n) * 2048 + 1024))
#define DPPMAX(x, ctl, rm) do { x = max(x, (int)__builtin_amdgcn_update_dpp((int)0x80000000, x, ctl, rm, 0xf, false)); KEEP(x); } while (0)
  for (int s = 0; s < NSEG; ++s) {
    const unsigned sM = (s & 3) * 16384;
    if (__builtin_expect(__builtin_amdgcn_ballot_w64(min(min4u(f0), min4u(f1)) == 0) != 0, 0)) {
      for (int spin = 0; spin < spin_limit; ++spin) {
        asm volatile("" ::: "memory");
        f0 = ld128(rX, vM, sM, 16); f1 = ld128(rX, vM, sM + 4096, 16); CNT(10);
        if (__builtin_amdgcn_ballot_w64(min(min4u(f0), min4u(f1)) == 0) == 0) break;
        if (spin == spin_limit - 1) spin_limit = 0;
      }
      asm volatile("s_nop 1" : "+v"(f0), "+v"(f1));
    }
    ST(0);
    bf16x8 af[8];
    af[0] = asb8(f0); af[1] = asb8(f1);
    *(LAS u32x4*)(lds + mst_st) = f0;
    *(LAS u32x4*)(lds + mst_st + 4096) = f1;
    FENCE();
    Q0(0);
    __builtin_amdgcn_raw_buffer_store_b128(__builtin_bit_cast(u32x4, av0), rX, vPz, ((s + 1) & 1) * 524288, 16);
    FENCE();
    Q1(0);
    __builtin_amdgcn_raw_buffer_store_b64(__builtin_bit_cast(u32x2, __builtin_shufflevector(av0, av0, 0, 1)), rX, vMz, ((s + 2) & 3) * 16384, 16);
    FENCE();
    Q0(1);
    __builtin_amdgcn_raw_buffer_store_b32((unsigned)s, rX, vCH, 0, 16);
    FENCE();
    Q1(1);
    lds_barrier();
    unsigned long long rt64;
    asm volatile("s_memrealtime %0" : "=s"(rt64) :: "memory");
    ST(1);
    bf16x16 wl[6];
    LDA(2); LDA(3); K0(0); LDA(4); LDA(5); K1(0); LDA(6); LDA(7); K0(1); K1(1);
    VV(0); V0(1);
    asm volatile("s_waitcnt lgkmcnt(0)" : "+s"(rt64) :: "memory");
    const unsigned rown = (unsigned)rt64;
    __builtin_amdgcn_raw_buffer_store_b32(rown, rX, vTSst, (s & 3) * 16384, 16);
    FENCE();
    V1(1);
    QQ(2); FENCE(); wl[0] = LDF(wv_ld, 0); FENCE();
    QQ(3); FENCE(); wl[1] = LDF(wv_ld, 1); FENCE();
    QQ(4); FENCE(); wl[2] = LDF(wv_ld, 2); FENCE();
    QQ(5); FENCE(); wl[3] = LDF(wv_ld, 3); FENCE();
    QQ(6); QQ(7);
    K0(2); K1(2); K0(3);
    asm volatile("s_nop 0" : "+v"(aq0), "+v"(aq1));
    {
      float t0 = aq0[0] + aq0[2], t1 = aq1[0] + aq1[2]; KEEP(t0); KEEP(t1);
      K1(3);
      float t2 = aq0[1] + aq0[3], t3 = aq1[1] + aq1[3]; KEEP(t2); KEEP(t3);
      K0(4);
      const unsigned d0 = pk2(t0, t1), d1 = pk2(t2, t3);
      *(LAS unsigned*)(lds + qi_st) = d0; *(LAS unsigned*)(lds + qi_st + 16) = d1;
      FENCE();
    }
    K1(4); K0(5); K1(5); K0(6);
    lds_barrier();
    TRS(tb2);
    ST(2);
    bf16x8 qf[4], kf[4];
    qf[0] = *(LAS bf16x8*)(lds + qf_ld); qf[1] = *(LAS bf16x8*)(lds + qf_ld + QI_W); FENCE();
    K1(6); FENCE();
    qf[2] = *(LAS bf16x8*)(lds + qf_ld + 2 * QI_W); qf[3] = *(LAS bf16x8*)(lds + qf_ld + 3 * QI_W); FENCE();
    K0(7); FENCE(); wl[4] = LDF(wv_ld, 4); FENCE();
    K1(7); FENCE(); wl[5] = LDF(wv_ld, 5); FENCE();
    V0(2); V1(2); V0(3);
    asm volatile("" : "+v"(ak0), "+v"(ak1));
    unsigned kd0, kd1;
    f32x4 sx0, sx1, sm;
    {
      float t0 = ak0[0] + ak0[2], t1 = ak1[0] + ak1[2]; KEEP(t0); KEEP(t1);
      V1(3);
      float t2 = ak0[1] + ak0[3], t3 = ak1[1] + ak1[3]; KEEP(t2); KEEP(t3);
      MF0m(sx0, kxf[0], qf[0]);
      kd0 = pk2(t0, t1); kd1 = pk2(t2, t3);
      *(LAS unsigned*)(lds + qi_st + 256) = kd0; *(LAS unsigned*)(lds + qi_st + 256 + 16) = kd1;
    }
    MF0m(sx1, kxf[1], qf[0]);
    MFm(sx0, kxf[2], qf[1]);
    int ts = (int)__builtin_amdgcn_raw_buffer_load_b32(rX, vTS, (s & 3) * 16384, 16);
    MFm(sx1, kxf[3], qf[1]);
    MFm(sx0, kxf[4], qf[2]); MFm(sx1, kxf[5], qf[2]);
    lds_barrier();
    TRS(tb3);
    ST(3);
    kf[0] = *(LAS bf16x8*)(lds + kf_ld); kf[1] = *(LAS bf16x8*)(lds + kf_ld + QI_W);
    MFm(sx0, kxf[6], qf[3]);
    kf[2] = *(LAS bf16x8*)(lds + kf_ld + 2 * QI_W); kf[3] = *(LAS bf16x8*)(lds + kf_ld + 3 * QI_W);
    MFm(sx1, kxf[7], qf[3]);
    const bf16x8 vx0 = *(LAS bf16x8*)(lds + vx_ld), vx1 = *(LAS bf16x8*)(lds + vx_ld + 1024);
    FENCE();
    bf16x16 wo[8];
    SMF_V(av0, af[6], wl[0]); SMF_V(av1, af[6], wl[1]); FENCE();
    wo[0] = LDF(wo_ld, 0);
    MF0m(sm, kf[0], qf[0]);
    wo[1] = LDF(wo_ld, 1);
    MFm(sm, kf[1], qf[1]);
    float mm = max3f(sx0[0], sx0[1], sx0[2]), m2 = max3f(sx0[3], sx1[0], sx1[1]); KEEP(mm); KEEP(m2);
    MFm(sm, kf[2], qf[2]);
    m2 = max3f(m2, sx1[2], sx1[3]); mm = max2f(mm, m2); KEEP(mm);
    MFm(sm, kf[3], qf[3]);
    u32x2 on_lo, on_hi;
    asm volatile("v_mov_b64 %0, %2\n\tv_mov_b64 %1, %2" : "=v"(on_lo), "=v"(on_hi) : "s"(0x3f803f803f803f80ull));
    const bf16x8 ones = asb8(__builtin_shufflevector(on_lo, on_hi, 0, 1, 2, 3));
    wo[4] = LDF(wo_ld, 4); FENCE();
    SMF_V(av0, af[7], wl[2]); FENCE(); wo[5] = LDF(wo_ld, 5); FENCE(); SMF_V(av1, af[7], wl[3]);
    SMF_V(av0, af[5], wl[4]);
    asm volatile("" : "+v"(sm));
    mm = max3f(mm, sm[0], sm[1]);
    { float t_;
      asm volatile("v_mov_b32 %1, %0\n\tv_smfmac_f32_16x16x64_bf16 %2, %4, %5, %8\n\ts_nop 0\n\tv_permlane32_swap_b32 %0, %1\n\tv_max_f32 %0, %0, %1\n\tv_mov_b32 %1, %0\n\t"
                   "v_smfmac_f32_16x16x64_bf16 %3, %6, %7, %8\n\ts_nop 0\n\tv_permlane16_swap_b32 %0, %1\n\tv_max_f32 %0, %0, %1"
                   : "+v"(mm), "=&v"(t_), "+v"(av1), "+v"(av0) : "v"(af[5]), "v"(wl[5]), "v"(af[4]), "a"(wv[0][4]), "v"(sidx)); }
    V1(4);
    const float mc = mm * CEXP;
    float ex[8];
#pragma unroll
    for (int j = 0; j < 4; ++j) { ex[j] = __builtin_amdgcn_exp2f(sx0[j] * CEXP - mc); ex[4 + j] = __builtin_amdgcn_exp2f(sx1[j] * CEXP - mc); }
    const u32x4 exp_ = {pk2(ex[0], ex[1]), pk2(ex[2], ex[3]), pk2(ex[4], ex[5]), pk2(ex[6], ex[7])};
    f32x4 ax, dn;
    u32x2 vv;
    { const bf16x8 b0 = asb8((u32x4){lo8 ? exp_.x : 0u, lo8 ? exp_.y : 0u, lo8 ? exp_.z : 0u, lo8 ? exp_.w : 0u});
      const bf16x8 ea = asb8(exp_);
      MFN0(ax, vx0, b0);
      float e0 = __builtin_amdgcn_exp2f(sm[0] * CEXP - mc); KEEP(e0);
      MF0(dn, ones, ea);
      float e1 = __builtin_amdgcn_exp2f(sm[1] * CEXP - mc); KEEP(e1);
      const bf16x8 b1 = asb8((u32x4){lo8 ? 0u : exp_.x, lo8 ? 0u : exp_.y, lo8 ? 0u : exp_.z, lo8 ? 0u : exp_.w});
      MFN(ax, vx1, b1);
      asm volatile("" : "+v"(av0), "+v"(av1));
      const unsigned em = pk2(e0, e1);
      vv = (u32x2){pk2(av0[0] + av0[2], av0[1] + av0[3]), pk2(av1[0] + av1[2], av1[1] + av1[3])};
      const u32x2 bm = {lo8 ? em : 0u, lo8 ? 0u : em};
      const bf16x4 on2 = __builtin_shufflevector(ones, ones, 0, 1, 2, 3);
      asm volatile("s_nop 1\n\tv_mfma_f32_16x16x16_bf16 %0, %2, %4, %0\n\tv_mfma_f32_16x16x16_bf16 %1, %3, %4, %1" : "+v"(dn), "+v"(ax) : "v"(on2), "v"(vv), "v"(bm) : "memory"); }
    f32x4 ap0 = zero4(), ap1 = zero4(), ap2 = zero4();
    asm volatile("s_nop 0" : "+v"(dn), "+v"(ax) :: "memory");
    const float rinv = __builtin_amdgcn_rcpf(dn[0]);
    {
      const u32x2 o = {pk2(ax[0] * rinv, ax[1] * rinv), pk2(ax[2] * rinv, ax[3] * rinv)};
      *(LAS u32x2*)(lds + oi_st) = o;
    }
    FENCE();
    asm volatile("v_subrev_u32 %0, %1, %0" : "+v"(ts) : "s"(rown) : "memory");
    f32x4 ap3 = zero4();
    __builtin_amdgcn_raw_buffer_store_b32(a ? vv.x : kd0, rKVM, vKV0, s * 2048, 16);
    __builtin_amdgcn_raw_buffer_store_b32(a ? vv.y : kd1, rKVM, vKV0, s * 2048 + kvd, 16);
    if (s == NSEG - 1) break;
    lds_barrier();
    TRS(tb4);
    ST(4);
    const bf16x8 o0 = *(LAS bf16x8*)(lds + L_MST + lane * 16), o1 = *(LAS bf16x8*)(lds + L_MST + 1024 + lane * 16);
    FENCE();
    wo[2] = LDF(wo_ld, 2); wo[3] = LDF(wo_ld, 3); wo[6] = LDF(wo_ld, 6); wo[7] = LDF(wo_ld, 7);
    FENCE();
    kv_issue(s + 1);
    FENCE();
    DPPMAX(ts, 0x111, 0xf); DPPMAX(ts, 0x112, 0xf);
    SMF_V(ap0, o0, wo[0]); DPPMAX(ts, 0x114, 0xf); SMF_V(ap1, o0, wo[1]); DPPMAX(ts, 0x118, 0xf); SMF_V(ap0, o1, wo[4]); DPPMAX(ts, 0x142, 0xa);
    SMF_V(ap1, o1, wo[5]); DPPMAX(ts, 0x143, 0xc);
    SMF_V(ap2, o0, wo[2]); SMF_V(ap3, o0, wo[3]); SMF_V(ap2, o1, wo[6]);
    asm volatile("" : "+v"(ap0), "+v"(ap1));
    u32x4 pv;
    { float t0 = ap0[0] + ap0[2], t1 = ap1[0] + ap1[2]; KEEP(t0); KEEP(t1);
      SMF_V(ap3, o1, wo[7]);
      pv.x = max(pk2(t0, t1), 1u); pv.z = max(pk2(ap0[1] + ap0[3], ap1[1] + ap1[3]), 1u); }
    asm volatile("s_nop 4" : "+v"(pv.x), "+v"(pv.z), "+v"(ap2), "+v"(ap3));
    pv.y = max(pk2(ap2[0] + ap2[2], ap3[0] + ap3[2]), 1u); pv.w = max(pk2(ap2[1] + ap2[3], ap3[1] + ap3[3]), 1u);
    const unsigned sP = (s & 1) * 524288;
    st128(pv, rX, vPst, sP);
    ST(5);
    if (wave == 3) {
      const int tsm = __builtin_amdgcn_readlane(ts, 63);
      int dlt = tsm;
      asm volatile("s_max_i32 %0, %0, 0\n\ts_min_i32 %0, %0, 100\n\ts_mul_i32 %0, %0, 3\n\ts_lshr_b32 %0, %0, 2" : "+s"(dlt) :: "scc");
      const bf16x8 asel = *(LAS bf16x8*)(lds + L_OI + lane * 16);
      FENCE();
      if (D1) __builtin_amdgcn_s_sleep(D1);
      spin_delay(TA + dlt);
      u32x4 r0 = ld128(rX, vPld, sP, 16), r1 = ld128(rX, vPld, sP + 1024, 16), r2 = ld128(rX, vPld, sP + 2048, 16), r3 = ld128(rX, vPld, sP + 3072, 16);
      if (__builtin_expect(__builtin_amdgcn_ballot_w64(min(min3u(min4u(r0), min4u(r1), min4u(r2)), min4u(r3)) == 0) != 0, 0)) {
        for (int spin = 0; spin < spin_limit; ++spin) {
          asm volatile("" ::: "memory");
          CNT(11);
          r0 = ld128(rX, vPld, sP, 16); r1 = ld128(rX, vPld, sP + 1024, 16); r2 = ld128(rX, vPld, sP + 2048, 16); r3 = ld128(rX, vPld, sP + 3072, 16);
          if (__builtin_amdgcn_ballot_w64(min(min3u(min4u(r0), min4u(r1), min4u(r2)), min4u(r3)) == 0) == 0) break;
          if (spin == spin_limit - 1) spin_limit = 0;
        }
      }
      ST(6);
      f32x4 d0;
      { const bf16x8 b0 = asb8(r0), b1 = asb8(r1), b2 = asb8(r2), b3 = asb8(r3);
        MFN0(d0, asel, b0); MF(d0, asel, b1); MF(d0, asel, b2); MF(d0, asel, b3); }
      PIN1(7, d0);
#pragma unroll
      for (int j = 0; j < 4; ++j) {
        float dj = d0[j];
        asm("v_add_f32_dpp %0, %0, %0 row_ror:8 row_mask:0xf bank_mask:0xf" : "+v"(dj));
        d0[j] = dj;
      }
      u32x2 mo = {max(pk2(d0[0], d0[1]), 1u), max(pk2(d0[2], d0[3]), 1u)};
      __builtin_amdgcn_raw_buffer_store_b64(mo, rX, vMst, ((s + 1) & 3) * 16384, 16);
      FENCE();
      ST(7);
      kv_commit();
      if (D2) __builtin_amdgcn_s_sleep(D2);
      spin_delay(TB);
    } else {
      kv_commit();
      if (wave == 0 && s + 2 < NSEG) {
        unsigned lo = vPz; asm volatile("" : "+v"(lo)); lo = (lo >> 4) & 63u;
        wait_flags(rX, lo < 4 ? (unsigned)(OFF_FLG + (F_KV + h) * 128) + lo * 4 : 0x80000000u, s + 3, spin_limit);
      }
    }
    lds_barrier();
    f0 = ld128(rX, vM, ((s + 1) & 3) * 16384, 16); f1 = ld128(rX, vM, ((s + 1) & 3) * 16384 + 4096, 16);
    FENCE();
    ST(8);
    aq0 = zero4(); aq1 = zero4(); ak0 = zero4(); ak1 = zero4(); av0 = zero4(); av1 = zero4();
#pragma unroll
    for (int n = 0; n < 8; ++n) kxf[n] = *(LAS bf16x8*)(lds + L_KX + n * 1024 + lane * 16);
    FENCE();
  }
  vmwait<0>();
  lds_barrier();
  __builtin_amdgcn_raw_buffer_store_b32((unsigned)NSEG, rX, vCH, 0, 16);
}

__global__ void __launch_bounds__(256, 1) k_main(Params p) {
  extern __shared__ __attribute__((aligned(16))) char smem[];
  LAS char* lds = (LAS char*)smem;
  const int b = blockIdx.x, x = b & 7, slot = b >> 3;
  if (x < 4) {
    if (slot < 16) chain_role(p, lds, x * 8 + (slot >> 1), slot & 1);
    else if (slot < 24) ah_role(p, lds, x * 8 + slot - 16);
    else red_role(p, lds, x * 8 + slot - 24);
  } else {
    const int fi = slot * 4 + (x - 4);
    if (fi < 32) proj_role<0, 4>(p, lds, fi);
    else if (fi < 48) proj_role<1, 4>(p, lds, fi - 32);
    else if (fi < 62) proj_role<3, 1>(p, lds, fi - 48);
    else if (fi < 69) proj_role<2, 2>(p, lds, fi - 62);
    else if (fi < 125) sp_role(p, lds, fi - 69);
  }
}

extern "C" void kernel_launch(void* const* d_in, const int* in_sizes, int n_in, void* d_out, int out_size, void* d_ws, size_t ws_size,
                              hipStream_t stream) {
  Params p;
  p.x = (const u16*)d_in[0]; p.mem0 = (const u16*)d_in[1]; p.Wq = (const u16*)d_in[2]; p.Wk = (const u16*)d_in[3];
  p.Wv = (const u16*)d_in[4]; p.Wo = (const u16*)d_in[5]; p.w_in = (const u16*)d_in[8]; p.w_sp = (const u16*)d_in[9];
  p.b_sp = (const u16*)d_in[10]; p.w_out = (const u16*)d_in[11]; p.out = (u16*)d_out;
  p.ws = (char*)(((uintptr_t)d_ws + (2u << 20) - 1) & ~(uintptr_t)((2u << 20) - 1));
  (void)hipFuncSetAttribute((const void*)k_main, hipFuncAttributeMaxDynamicSharedMemorySize, 163840);
  hipLaunchKernelGGL(k0_prep, dim3(258), dim3(256), 0, stream, p);
  hipLaunchKernelGGL(k_main, dim3(256), dim3(256), 163840, stream, p);
}
```

```cpp
#include <hip/hip_runtime.h>
#include <stdint.h>
#include <type_traits>

typedef __bf16 bf16x2 __attribute__((ext_vector_type(2)));
typedef __bf16 bf16x4 __attribute__((ext_vector_type(4)));
typedef __bf16 bf16x8 __attribute__((ext_vector_type(8)));
typedef __bf16 bf16x16 __attribute__((ext_vector_type(16)));
typedef float f32x4 __attribute__((ext_vector_type(4)));
typedef float f32x2 __attribute__((ext_vector_type(2)));
typedef unsigned u32x4 __attribute__((ext_vector_type(4)));
typedef unsigned u32x2 __attribute__((ext_vector_type(2)));
typedef unsigned short u16;
#define LAS __attribute__((address_space(3)))
#define GAS __attribute__((address_space(1)))
typedef __amdgpu_buffer_rsrc_t rsrc_t;

constexpr int S_ = 2048, DM = 512, NH = 32, DH = 128, NSEG = 64, FH = 896;
constexpr size_t OFF_X = 0;
constexpr size_t X_M = 1u << 20;
constexpr size_t X_TS = X_M + 65536;
constexpr size_t OFF_FLG = 2u << 20;
constexpr size_t OFF_XN = 4u << 20;
constexpr size_t OFF_KF = 8u << 20;
constexpr size_t OFF_VF = 24u << 20;
constexpr size_t OFF_QF = 40u << 20;
constexpr size_t OFF_KM = 56u << 20;
constexpr size_t OFF_VM = 60u << 20;
constexpr size_t OFF_Z1 = 64u << 20;
constexpr size_t OFF_Z2 = 68u << 20;
constexpr size_t OFF_ZN = 72u << 20;
constexpr size_t OFF_SL = 128u << 20;
constexpr int NSLAB = 39;
constexpr int FLG_SEL = 0;
constexpr int FLG_BYTES = 65536;
constexpr int F_KV = 2, F_Q = 34, F_CH = 72, F_AH = 136, F_LN = 168, F_SP = 232, F_Z2 = 288, F_Z1 = 344;
constexpr int SPIN_LIMIT = 200000;

__device__ __forceinline__ rsrc_t mkrsrc(const void* p, unsigned bytes) { return __builtin_amdgcn_make_buffer_rsrc((void*)p, 0, bytes, 0x00020000); }
__device__ __forceinline__ u32x4 ld128(rsrc_t r, unsigned v, unsigned s, int aux) {
  return aux ? __builtin_amdgcn_raw_buffer_load_b128(r, v, s, 16) : __builtin_amdgcn_raw_buffer_load_b128(r, v, s, 0);
}
__device__ __forceinline__ void st128(u32x4 v, rsrc_t r, unsigned vo, unsigned so) {
  __builtin_amdgcn_raw_buffer_store_b128(v, r, vo, so, 16);
  asm volatile("s_nop 1" :: "v"(v));
}
__device__ __forceinline__ unsigned pk2(float a, float b) {
  bf16x2 v = {(__bf16)a, (__bf16)b};
  return __builtin_bit_cast(unsigned, v);
}
__device__ __forceinline__ float bflo(unsigned u) { return __builtin_bit_cast(float, u << 16); }
__device__ __forceinline__ float bfhi(unsigned u) { return __builtin_bit_cast(float, u & 0xffff0000u); }
__device__ __forceinline__ void lds_barrier() { asm volatile("s_waitcnt lgkmcnt(0)\n\ts_barrier" ::: "memory"); }
__device__ __forceinline__ bf16x4 trrd(LAS char* p) { return __builtin_amdgcn_ds_read_tr16_b64_v4bf16((LAS bf16x4*)p); }
__device__ __forceinline__ bf16x8 cat8(bf16x4 a, bf16x4 b) { return __builtin_shufflevector(a, b, 0, 1, 2, 3, 4, 5, 6, 7); }
__device__ __forceinline__ bf16x16 cat16(bf16x8 a, bf16x8 b) { return __builtin_shufflevector(a, b, 0, 1, 2, 3, 4, 5, 6, 7, 8, 9, 10, 11, 12, 13, 14, 15); }
__device__ __forceinline__ bf16x8 asb8(u32x4 v) { return __builtin_bit_cast(bf16x8, v); }
__device__ __forceinline__ f32x4 mfma16(bf16x8 a, bf16x8 b, f32x4 c) { return __builtin_amdgcn_mfma_f32_16x16x32_bf16(a, b, c, 0, 0, 0); }
__device__ __forceinline__ f32x4 smfma(bf16x8 a, bf16x16 b, f32x4 c, int idx) { return __builtin_amdgcn_smfmac_f32_16x16x64_bf16(a, b, c, idx, 0, 0); }
__device__ __forceinline__ void glds16(const void* g, unsigned ldsoff) {
  unsigned keep;
  asm volatile("s_mov_b32 %0, m0\n\ts_mov_b32 m0, %2\n\ts_nop 0\n\tglobal_load_lds_dwordx4 %1, off\n\ts_mov_b32 m0, %0" : "=&s"(keep) : "v"(g), "s"(ldsoff) : "memory");
}
template <int IMM> __device__ __forceinline__ void glds_s(unsigned voff, const void* sbase, unsigned m0v) {
  asm volatile("s_mov_b32 m0, %2\n\ts_nop 0\n\tglobal_load_lds_dwordx4 %0, %1 offset:%3" :: "v"(voff), "s"(sbase), "s"(m0v), "i"(IMM));
}
#define SB() __builtin_amdgcn_sched_barrier(0)
#define SMF_A(acc, a, b) asm volatile("v_smfmac_f32_16x16x64_bf16 %0, %1, %2, %3" : "+v"(acc) : "v"(a), "a"(b), "v"(sidx))
#define SMF_V(acc, a, b) asm volatile("v_smfmac_f32_16x16x64_bf16 %0, %1, %2, %3" : "+v"(acc) : "v"(a), "v"(b), "v"(sidx))
#define MF(acc, a, b) asm volatile("v_mfma_f32_16x16x32_bf16 %0, %1, %2, %0" : "+v"(acc) : "v"(a), "v"(b))
#define MF0(acc, a, b) asm volatile("v_mfma_f32_16x16x32_bf16 %0, %1, %2, 0" : "=&v"(acc) : "v"(a), "v"(b))
#define MFN(acc, a, b) asm volatile("s_nop 1\n\tv_mfma_f32_16x16x32_bf16 %0, %1, %2, %0" : "+v"(acc) : "v"(a), "v"(b))
#define MFN0(acc, a, b) asm volatile("s_nop 1\n\tv_mfma_f32_16x16x32_bf16 %0, %1, %2, 0" : "=&v"(acc) : "v"(a), "v"(b))
#define MFm(acc, a, b) asm volatile("v_mfma_f32_16x16x32_bf16 %0, %1, %2, %0" : "+v"(acc) : "v"(a), "v"(b) : "memory")
#define MF0m(acc, a, b) asm volatile("v_mfma_f32_16x16x32_bf16 %0, %1, %2, 0" : "=&v"(acc) : "v"(a), "v"(b) : "memory")
__device__ __forceinline__ f32x4 zero4() {
  f32x2 lo, hi;
  asm volatile("v_mov_b64 %0, 0\n\tv_mov_b64 %1, 0" : "=v"(lo), "=v"(hi));
  return __builtin_shufflevector(lo, hi, 0, 1, 2, 3);
}
#define PIN1(n, x) asm volatile("s_nop " #n : "+v"(x))
#define PIN2(n, x, y) asm volatile("s_nop " #n : "+v"(x), "+v"(y))
#define PIN4(n, x, y, z, w) asm volatile("s_nop " #n : "+v"(x), "+v"(y), "+v"(z), "+v"(w))
__device__ __forceinline__ unsigned clk32() { unsigned long long t; asm volatile("s_memtime %0\n\ts_waitcnt lgkmcnt(0)" : "=s"(t) :: "memory"); return (unsigned)t; }
__device__ __forceinline__ void wait_clk(unsigned t0, int n) {
  asm volatile("s_add_i32 %0, %0, %1\n1:\n\ts_memtime vcc\n\ts_waitcnt lgkmcnt(0)\n\ts_sub_i32 %1, %0, vcc_lo\n\ts_cmp_gt_i32 %1, 0\n\ts_cbranch_scc1 1b"
               : "+s"(t0), "+s"(n) :: "scc", "vcc", "memory");
}
__device__ __forceinline__ float max3f(float a, float b, float c) { float r; asm("v_max3_f32 %0, %1, %2, %3" : "=v"(r) : "v"(a), "v"(b), "v"(c)); return r; }
__device__ __forceinline__ float max2f(float a, float b) { float r; asm("v_max_f32 %0, %1, %2" : "=v"(r) : "v"(a), "v"(b)); return r; }
__device__ __forceinline__ void swap32(float x, float& r0, float& r1) {
  asm("v_mov_b32 %0, %2\n\tv_mov_b32 %1, %2\n\ts_nop 1\n\tv_permlane32_swap_b32 %0, %1" : "=&v"(r0), "=&v"(r1) : "v"(x));
}
__device__ __forceinline__ void swap16(float x, float& r0, float& r1) {
  asm("v_mov_b32 %0, %2\n\tv_mov_b32 %1, %2\n\ts_nop 1\n\tv_permlane16_swap_b32 %0, %1" : "=&v"(r0), "=&v"(r1) : "v"(x));
}
__device__ __forceinline__ float xmax_g(float x) {
  float t;
  asm("v_mov_b32 %1, %0\n\ts_nop 1\n\tv_permlane32_swap_b32 %0, %1\n\tv_max_f32 %0, %0, %1\n\tv_mov_b32 %1, %0\n\ts_nop 1\n\tv_permlane16_swap_b32 %0, %1\n\tv_max_f32 %0, %0, %1"
      : "+v"(x), "=&v"(t));
  return x;
}
__device__ __forceinline__ float xsum_g(float x) {
  float t;
  asm("v_mov_b32 %1, %0\n\ts_nop 1\n\tv_permlane32_swap_b32 %0, %1\n\tv_add_f32 %0, %0, %1\n\tv_mov_b32 %1, %0\n\ts_nop 1\n\tv_permlane16_swap_b32 %0, %1\n\tv_add_f32 %0, %0, %1"
      : "+v"(x), "=&v"(t));
  return x;
}

struct Params {
  const u16* x; const u16* mem0; const u16* Wq; const u16* Wk; const u16* Wv; const u16* Wo;
  const u16* w_in; const u16* w_sp; const u16* b_sp; const u16* w_out;
  u16* out; char* ws;
};

__global__ void __launch_bounds__(256) k0_prep(Params p) {
  const int tid = threadIdx.x, lane = tid & 63, wave = tid >> 6, b = blockIdx.x;
  char* ws = p.ws;
  if (b < 256) {
#pragma unroll
    for (int rr = 0; rr < 2; ++rr) {
      const int row = b * 8 + wave * 2 + rr;
      const u32x4 v = *(const u32x4*)(p.x + (size_t)row * DM + lane * 8);
      float f[8] = {bflo(v.x), bfhi(v.x), bflo(v.y), bfhi(v.y), bflo(v.z), bfhi(v.z), bflo(v.w), bfhi(v.w)};
      float s = 0.f;
#pragma unroll
      for (int j = 0; j < 8; ++j) s += f[j];
#pragma unroll
      for (int m = 1; m < 64; m <<= 1) s += __shfl_xor(s, m);
      const float mean = s * (1.f / DM);
      float q = 0.f;
#pragma unroll
      for (int j = 0; j < 8; ++j) { f[j] -= mean; q += f[j] * f[j]; }
#pragma unroll
      for (int m = 1; m < 64; m <<= 1) q += __shfl_xor(q, m);
      const float rs = 1.f / sqrtf(q * (1.f / DM) + 1e-5f);
      u32x4 o = {pk2(f[0] * rs, f[1] * rs), pk2(f[2] * rs, f[3] * rs), pk2(f[4] * rs, f[5] * rs), pk2(f[6] * rs, f[7] * rs)};
      const int seg = row >> 5, tau = row & 31, T = (tau >> 2) & 1, i = 4 * (tau >> 3) + (tau & 3);
      *(u32x4*)(ws + OFF_XN + ((size_t)(seg * 2 + T) * 16 + (lane >> 2)) * 1024 + ((lane & 3) * 16 + i) * 16) = o;
    }
    *(u32x4*)(ws + OFF_X + (size_t)b * 4096 + tid * 16) = (u32x4){0, 0, 0, 0};
    if (b < 28) *(u32x4*)(ws + OFF_X + X_M + 16384 + (size_t)b * 4096 + tid * 16) = (u32x4){0, 0, 0, 0};
    if (b >= 32 && b < 48) *(u32x4*)(ws + OFF_FLG + 256 + (size_t)(b - 32) * 4080 + (tid < 255 ? tid : 254) * 16) = (u32x4){0, 0, 0, 0};
  } else if (b == 256) {
    for (int d = tid; d < 2048; d += 256) {
      const int line = d >> 5, rp = (d >> 4) & 1, cc = (d >> 1) & 7, t0 = (d & 1) * 2;
      const int bb = line & 1, g = (line >> 1) & 3, s = line >> 3;
      const int r = 2 * g + rp, k0 = 64 * s + 16 * t0 + 8 * bb + cc;
      unsigned v = (unsigned)p.mem0[r * DM + k0] | ((unsigned)p.mem0[r * DM + k0 + 16] << 16);
      v = v > 1u ? v : 1u;
      *(unsigned*)(ws + OFF_X + X_M + d * 4) = v;
      *(unsigned*)(ws + OFF_X + X_M + 8192 + d * 4) = v;
    }
  } else {
    if (tid == 0) {
      const char* base = ws + OFF_X + X_M + 0x20000;
      unsigned best[2] = {~0u, ~0u}; unsigned sink = 0;
      asm volatile("global_load_dword %0, %1, off sc1\n\ts_waitcnt vmcnt(0)" : "=&v"(sink) : "v"(base + 0x10000) : "memory");
      for (int r = 0; r < 4; ++r)
        for (int par = 0; par < 2; ++par) {
          const char* a = base + r * 16384 + par * 8192 + 1024; unsigned x;
          unsigned t0 = clk32();
          asm volatile("global_load_dword %0, %1, off sc1\n\ts_waitcnt vmcnt(0)" : "=&v"(x) : "v"(a) : "memory");
          unsigned t1 = clk32();
          sink += x & 0;
          best[par] = min(best[par], t1 - t0 + (x & 0));
        }
      const unsigned xcc = (unsigned)__builtin_amdgcn_s_getreg(20 | (3 << 11)) & 0xfu;
      unsigned sel = (best[1] < best[0]) ? 1u : 0u;
      sel ^= (xcc >> 2) & 1u;
      unsigned* f = (unsigned*)(ws + OFF_FLG);
      f[FLG_SEL] = sel; f[1] = best[0]; f[2] = best[1]; f[3] = xcc;
    }
  }
}

__device__ __forceinline__ float gelu_tanh(float x) {
  const float u = 0.7978845608028654f * (x + 0.044715f * x * x * x);
  const float e = __builtin_amdgcn_exp2f(u * 2.885390081777927f);
  const float th = 1.f - 2.f * __builtin_amdgcn_rcpf(e + 1.f);
  return 0.5f * x * (1.f + th);
}

#define MAPV 1
#define PSL 2
__device__ __forceinline__ void wait_flags(rsrc_t rF, unsigned off, unsigned want, int& budget) {
  const bool act = off != 0x80000000u;
  for (int spin = 0; spin < budget; ++spin) {
    const unsigned v = __builtin_amdgcn_raw_buffer_load_b32(rF, off, 0, 16);
    if (__builtin_amdgcn_ballot_w64(act && v < want) == 0) return;
    __builtin_amdgcn_s_sleep(PSL);
    asm volatile("" ::: "memory");
  }
  budget = 0;
}
__device__ __forceinline__ void put_flag(rsrc_t rF, unsigned off, unsigned v, int lane) {
  __builtin_amdgcn_raw_buffer_store_b32(v, rF, lane == 0 ? off : 0x80000000u, 0, 16);
}
template <int N> __device__ __forceinline__ void vmwait() { asm volatile("s_waitcnt vmcnt(%0)" :: "i"(N) : "memory"); }

template <int KIND, int NT>
__device__ __forceinline__ void proj_role(const Params& p, LAS char* lds, int j, int s0, int s1, int fline) {
  const int tid = threadIdx.x, lane = tid & 63, wave = __builtin_amdgcn_readfirstlane(tid >> 6);
  const int g = lane >> 4, c = lane & 15;
  char* ws = p.ws;
  const unsigned lbase = (unsigned)(uintptr_t)lds;
  constexpr int NG = 4 * NT, RB = 128 * NT, LPR = 8 * NT, RPI = 8 / NT;
  const char* xn = ws + OFF_XN;
  bf16x8 w[NT][16];
  const int ldw = (KIND >= 2) ? 2 * FH : NH * DH;
  auto rotf = [](int row) { return NT == 1 ? ((row >> 1) & 1) + 2 * ((row >> 3) & 1) : (row & 3) + 4 * ((row >> 3) & 1); };
#pragma unroll
  for (int half = 0; half < 2; ++half) {
    if (half) lds_barrier();
#pragma unroll 4
    for (int n = 0; n < 8 * NT; ++n) {
      const int row = (wave * 8 * NT + n) * RPI + lane / LPR;
      const int x = lane % LPR;
      const int G = ((x >> 1) - rotf(row)) & (NG - 1);
      const int k = half * 256 + row;
      const u16* src;
      if (KIND == 0) src = (G < 8 ? p.Wk : p.Wv) + (size_t)k * ldw + j * 128 + (G & 7) * 16 + (x & 1) * 8;
      else if (KIND == 1) src = p.Wq + (size_t)k * ldw + j * 256 + G * 16 + (x & 1) * 8;
      else if (KIND == 2) src = p.w_in + (size_t)k * ldw + j * 128 + G * 16 + (x & 1) * 8;
      else src = p.w_in + (size_t)k * ldw + FH + j * 64 + G * 16 + (x & 1) * 8;
      glds16(src, lbase + (wave * 8 * NT + n) * 1024);
    }
    vmwait<0>();
    lds_barrier();
#pragma unroll
    for (int ks = 0; ks < 8; ++ks)
#pragma unroll
      for (int t = 0; t < NT; ++t) {
        bf16x4 pc[2];
#pragma unroll
        for (int u = 0; u < 2; ++u) {
          const int row = 32 * ks + 8 * g + 4 * u + (c >> 2);
          pc[u] = trrd(lds + row * RB + ((wave * NT + t + rotf(row)) & (NG - 1)) * 32 + (c & 3) * 8);
        }
        w[t][half * 8 + ks] = cat8(pc[0], pc[1]);
      }
  }
  lds_barrier();
  const unsigned l16 = lane * 16;
  auto piece = [&](int s, int n) {
    const char* sb = xn + (size_t)s * 32768 + wave * 8192 + (n >> 2) * 4096;
    const unsigned m0v = lbase + (s % 3) * 32768 + wave * 8192 + (n >> 2) * 4096;
    if ((n & 3) == 0) glds_s<0>(l16, sb, m0v); else if ((n & 3) == 1) glds_s<1024>(l16, sb, m0v); else if ((n & 3) == 2) glds_s<2048>(l16, sb, m0v); else glds_s<3072>(l16, sb, m0v);
  };
#pragma unroll
  for (int n = 0; n < 8; ++n) piece(s0, n);
#pragma unroll
  for (int n = 0; n < 8; ++n) piece(s0 + 1, n);
  const rsrc_t rF = mkrsrc(ws + OFF_FLG, FLG_BYTES);
  const unsigned foff = fline * 128 + wave * 4;
  const rsrc_t rO = KIND == 0 ? mkrsrc(ws + (wave >= 2 ? OFF_VF : OFF_KF) + (size_t)j * NSEG * 8192 + (wave & 1) * 4096, NSEG * 8192)
                  : KIND == 1 ? mkrsrc(ws + OFF_QF + (size_t)(2 * j + (wave >> 1)) * NSEG * 8192 + (wave & 1) * 4096, NSEG * 8192)
                  : KIND == 2 ? mkrsrc(ws + OFF_Z1 + (size_t)(j * 4 + wave) * 1024, NSEG * 57344) : mkrsrc(ws + OFF_Z2 + (size_t)(j * 4 + wave) * 1024, NSEG * 57344);
  auto run = [&](auto vf) {
    constexpr bool vform = decltype(vf)::value;
    auto emit = [&](f32x4 (&prv)[2][NT], int s, int ch) {
      if (KIND >= 2) {
#pragma unroll
        for (int q = 0; q < NT; ++q) { const int e = ch * NT + q; prv[e / (4 * NT)][(e / 4) % NT][e & 3] = gelu_tanh(prv[e / (4 * NT)][(e / 4) % NT][e & 3]); }
        if (KIND == 2 && (ch == 3 || ch == 7)) {
          const int T = ch >> 2; const f32x4 a0 = prv[T][0], a1 = prv[T][NT - 1];
          st128((u32x4){pk2(a0[0], a1[0]), pk2(a0[1], a1[1]), pk2(a0[2], a1[2]), pk2(a0[3], a1[3])}, rO, l16, s * 57344 + T * 28672);
        }
        if (KIND == 3 && ch == 7)
          st128((u32x4){pk2(prv[0][0][0], prv[0][0][1]), pk2(prv[0][0][2], prv[0][0][3]), pk2(prv[1][0][0], prv[1][0][1]), pk2(prv[1][0][2], prv[1][0][3])}, rO, l16, s * 57344);
      } else if (ch < 4) {
        const int t = ch % NT;
        u32x4 o;
        if (vform) o = (u32x4){pk2(prv[0][t][0], prv[0][t][1]), pk2(prv[0][t][2], prv[0][t][3]), pk2(prv[1][t][0], prv[1][t][1]), pk2(prv[1][t][2], prv[1][t][3])};
        else { const f32x4 a0 = prv[ch & 1][(ch & 2) % NT], a1 = prv[ch & 1][((ch & 2) + 1) % NT];
               o = (u32x4){pk2(a0[0], a1[0]), pk2(a0[1], a1[1]), pk2(a0[2], a1[2]), pk2(a0[3], a1[3])}; }
        st128(o, rO, l16, s * 8192 + ch * 1024);
      }
    };
    auto step = [&](f32x4 (&acc)[2][NT], f32x4 (&prv)[2][NT], int s) {
      const int r = s - s0;
      if (r < 2) vmwait<8>(); else if (r >= 4 && s + 1 < s1) vmwait<9 + NT>(); else vmwait<0>();
      lds_barrier();
      if (r >= 2) put_flag(rF, foff, (r < 4 || s + 1 >= s1) ? s - 1 : s - 2, lane);
      LAS char* src = lds + (s % 3) * 32768 + lane * 16;
      bf16x8 xf[2][4];
#pragma unroll
      for (int q = 0; q < 4; ++q) xf[0][q] = *(LAS bf16x8*)(src + ((q & 1) * 16 + (q >> 1)) * 1024);
#pragma unroll
      for (int T = 0; T < 2; ++T)
#pragma unroll
        for (int t = 0; t < NT; ++t) acc[T][t] = (f32x4){};
      const int sp = r > 0 ? s - 1 : s;
      const int sn = s + 2 < s1 ? s + 2 : s;
#pragma unroll
      for (int ch = 0; ch < 8; ++ch) {
        SB();
        if (ch < 7) {
#pragma unroll
          for (int q = 0; q < 4; ++q) xf[(ch + 1) & 1][q] = *(LAS bf16x8*)(src + ((q & 1) * 16 + 2 * (ch + 1) + (q >> 1)) * 1024);
        }
        SB();
#pragma unroll
        for (int q = 0; q < 4; ++q)
#pragma unroll
          for (int t = 0; t < NT; ++t)
            acc[q & 1][t] = vform ? mfma16(xf[ch & 1][q], w[t][2 * ch + (q >> 1)], acc[q & 1][t]) : mfma16(w[t][2 * ch + (q >> 1)], xf[ch & 1][q], acc[q & 1][t]);
        if (r > 0) emit(prv, sp, ch);
        if (s + 2 < s1) piece(sn, ch);
      }
      SB();
    };
    f32x4 accA[2][NT], accB[2][NT] = {};
    int s = s0;
    for (; s + 1 < s1; s += 2) { step(accA, accB, s); step(accB, accA, s + 1); }
    if (s < s1) {
      step(accA, accB, s);
#pragma unroll
      for (int ch = 0; ch < 8; ++ch) emit(accA, s1 - 1, ch);
    } else {
#pragma unroll
      for (int ch = 0; ch < 8; ++ch) emit(accB, s1 - 1, ch);
    }
  };
  if ((KIND == 0 && wave >= 2) || KIND == 3) run(std::true_type{}); else run(std::false_type{});
  vmwait<0>();
  put_flag(rF, foff, s1, lane);
  lds_barrier();
}

__device__ __forceinline__ float xmax_g(float x);
__device__ __forceinline__ float xsum_g(float x);
__device__ __forceinline__ void ah_role(const Params& p, LAS char* lds, int h) {
  const int tid = threadIdx.x, lane = tid & 63, wave = __builtin_amdgcn_readfirstlane(tid >> 6);
  const int g = lane >> 4, c = lane & 15;
  char* ws = p.ws;
  const unsigned lbase = (unsigned)(uintptr_t)lds;
#pragma unroll 4
  for (int n = 0; n < 32; ++n) {
    const int e = wave * 32 + n;
    const int G = ((lane >> 2) - (e & 3)) & 15;
    glds16(p.Wo + (size_t)(h * DH + e) * DM + G * 32 + (lane & 3) * 8, lbase + e * 1024);
  }
  vmwait<0>();
  lds_barrier();
  bf16x8 wo[4][2][4];
#pragma unroll
  for (int P = 0; P < 4; ++P)
#pragma unroll
    for (int t = 0; t < 2; ++t)
#pragma unroll
      for (int m = 0; m < 4; ++m) {
        bf16x4 pc[2];
#pragma unroll
        for (int u = 0; u < 2; ++u) {
          const int e = 32 * m + 16 * u + 4 * g + (c >> 2);
          pc[u] = trrd(lds + e * 1024 + ((wave * 4 + P + (e & 3)) & 15) * 64 + (c & 3) * 16 + t * 8);
        }
        wo[P][t][m] = cat8(pc[0], pc[1]);
      }
  lds_barrier();
  const rsrc_t rF = mkrsrc(ws + OFF_FLG, FLG_BYTES);
  const rsrc_t rK = mkrsrc(ws + OFF_KF + (size_t)h * NSEG * 8192, NSEG * 8192);
  const rsrc_t rV = mkrsrc(ws + OFF_VF + (size_t)h * NSEG * 8192, NSEG * 8192);
  const rsrc_t rQ = mkrsrc(ws + OFF_QF + (size_t)h * NSEG * 8192, NSEG * 8192);
  const rsrc_t rKM = mkrsrc(ws + OFF_KM + (size_t)h * NSEG * 2048, NSEG * 2048);
  const rsrc_t rVM = mkrsrc(ws + OFF_VM + (size_t)h * NSEG * 2048, NSEG * 2048);
  const rsrc_t rS = mkrsrc(ws + OFF_SL, NSEG * NSLAB * 32768);
  const int T = wave & 1, eh = wave >> 1;
  const unsigned fAll = lane < 4 ? (unsigned)((F_KV + h) * 128 + lane * 4) : lane < 6 ? (unsigned)((F_Q + (h >> 1)) * 128 + ((h & 1) * 2 + lane - 4) * 4)
                      : lane < 8 ? (unsigned)((F_CH + 2 * h + lane - 6) * 128) : 0x80000000u;
  const int tq = 8 * (c >> 2) + 4 * T + (c & 3);
  const float CEXP = 0.08838834764831845f * 1.4426950408889634f;
  int budget = SPIN_LIMIT;
  u32x4 pf[6];
  auto issue = [&](int s) {
    const unsigned so = s * 8192 + wave * 2048;
    pf[0] = ld128(rK, lane * 16, so, 16); pf[1] = ld128(rK, lane * 16, so + 1024, 16);
    pf[2] = ld128(rQ, lane * 16, so, 16); pf[3] = ld128(rQ, lane * 16, so + 1024, 16);
    pf[4] = ld128(rV, lane * 16, so, 16); pf[5] = ld128(rV, lane * 16, so + 1024, 16);
  };
  auto commit = [&](int s) {
    LAS char* d = lds + 16384 + (s & 1) * 24576 + wave * 2048 + lane * 16;
#pragma unroll
    for (int n = 0; n < 6; ++n) *(LAS u32x4*)(d + (n >> 1) * 8192 + (n & 1) * 1024) = pf[n];
  };
  u32x4 km[4]; unsigned vm[4];
  auto memfetch = [&](int s) {
#pragma unroll
    for (int k4 = 0; k4 < 4; ++k4) km[k4] = ld128(rKM, g * 128 + (2 * (c >> 2) + (c & 1)) * 16, s * 2048 + k4 * 512, 16);
#pragma unroll
    for (int n = 0; n < 4; ++n) vm[n] = __builtin_amdgcn_raw_buffer_load_b32(rVM, lane * 4, s * 2048 + (eh * 4 + n) * 256, 16);
  };
  wait_flags(rF, lane < 6 ? fAll : 0x80000000u, 2, budget);
  issue(0); commit(0); issue(1);
  lds_barrier();
  bool have = false;
  unsigned fl = 0;
#define AS(i)
  for (int s = 0; s < NSEG; ++s) {
    LAS char* in = lds + 16384 + (s & 1) * 24576 + lane * 16;
    bf16x8 qx[4], kx[8], vx[4];
#pragma unroll
    for (int k4 = 0; k4 < 4; ++k4) {
      qx[k4] = *(LAS bf16x8*)(in + 8192 + (k4 * 2 + T) * 1024);
      kx[2 * k4] = *(LAS bf16x8*)(in + (k4 * 2) * 1024); kx[2 * k4 + 1] = *(LAS bf16x8*)(in + (k4 * 2 + 1) * 1024);
    }
#pragma unroll
    for (int n = 0; n < 4; ++n) vx[n] = *(LAS bf16x8*)(in + 16384 + (eh * 4 + n) * 1024);
    SB();
    f32x4 sx[2] = {}, sm = {};
#pragma unroll
    for (int k4 = 0; k4 < 4; ++k4) { sx[0] = mfma16(kx[2 * k4], qx[k4], sx[0]); sx[1] = mfma16(kx[2 * k4 + 1], qx[k4], sx[1]); }
    if (!have) {
      vmwait<0>();
      if (s) put_flag(rF, (F_AH + h) * 128 + wave * 4, s, lane);
      for (int spin = 0; spin < budget; ++spin) {
        const unsigned f2 = __builtin_amdgcn_raw_buffer_load_b32(rF, fAll, 0, 16);
        if (__builtin_amdgcn_ballot_w64(lane >= 6 && lane < 8 && f2 < (unsigned)(s + 1)) == 0) break;
        __builtin_amdgcn_s_sleep(PSL);
        asm volatile("" ::: "memory");
        if (spin == budget - 1) budget = 0;
      }
      memfetch(s);
      asm volatile("" : "+v"(km[0]), "+v"(km[1]), "+v"(km[2]), "+v"(km[3]), "+v"(vm[0]), "+v"(vm[1]), "+v"(vm[2]), "+v"(vm[3]));
    }
    AS(0);
    float v[10];
#pragma unroll
    for (int T2 = 0; T2 < 2; ++T2)
#pragma unroll
      for (int r = 0; r < 4; ++r) v[T2 * 4 + r] = (8 * g + 4 * T2 + r <= tq) ? sx[T2][r] : -3.0e38f;
#pragma unroll
    for (int k4 = 0; k4 < 4; ++k4) sm = mfma16(asb8(km[k4]), qx[k4], sm);
    v[8] = sm[0]; v[9] = sm[1];
    float mm = v[0];
#pragma unroll
    for (int r = 1; r < 10; ++r) mm = fmaxf(mm, v[r]);
    mm = xmax_g(mm);
    float den = 0.f;
#pragma unroll
    for (int r = 0; r < 10; ++r) { v[r] = __builtin_amdgcn_exp2f((v[r] - mm) * CEXP); den += v[r]; }
    const float rinv = __builtin_amdgcn_rcpf(xsum_g(den));
    const bf16x8 pb = asb8((u32x4){pk2(v[0] * rinv, v[1] * rinv), pk2(v[2] * rinv, v[3] * rinv), pk2(v[4] * rinv, v[5] * rinv), pk2(v[6] * rinv, v[7] * rinv)});
    const bf16x8 pm = asb8((u32x4){pk2(v[8] * rinv, v[9] * rinv), 0, 0, 0});
    f32x4 o[4];
#pragma unroll
    for (int n = 0; n < 4; ++n) {
      o[n] = mfma16(vx[n], pb, (f32x4){});
      o[n] = mfma16(asb8((u32x4){vm[n], 0, 0, 0}), pm, o[n]);
    }
    LAS char* ob = lds + (s & 1) * 8192;
#pragma unroll
    for (int m2 = 0; m2 < 2; ++m2)
      *(LAS u32x4*)(ob + (T * 4 + eh * 2 + m2) * 1024 + lane * 16) =
          (u32x4){pk2(o[2 * m2][0], o[2 * m2][1]), pk2(o[2 * m2][2], o[2 * m2][3]), pk2(o[2 * m2 + 1][0], o[2 * m2 + 1][1]), pk2(o[2 * m2 + 1][2], o[2 * m2 + 1][3])};
    AS(1);
    const bool pub = have;
    asm volatile("s_waitcnt vmcnt(0)" : "+v"(fl) :: "memory");
    have = false;
    AS(2);
    if (s + 1 < NSEG) commit(s + 1);
    asm volatile("" ::: "memory");
    if (pub) put_flag(rF, (F_AH + h) * 128 + wave * 4, s, lane);
    if (s + 1 < NSEG) {
      AS(6);
      have = __builtin_amdgcn_ballot_w64(lane >= 6 && lane < 8 && fl < (unsigned)(s + 2)) == 0;
      if (have) memfetch(s + 1);
      if (s + 2 < NSEG) {
        if (__builtin_amdgcn_ballot_w64(lane < 6 && fl < (unsigned)(s + 3)) != 0) wait_flags(rF, lane < 6 ? fAll : 0x80000000u, s + 3, budget);
        issue(s + 2);
      }
      fl = __builtin_amdgcn_raw_buffer_load_b32(rF, fAll, 0, 16);
    }
    AS(7);
    lds_barrier();
    AS(3);
    bf16x8 of[8];
#pragma unroll
    for (int n = 0; n < 8; ++n) of[n] = *(LAS bf16x8*)(ob + n * 1024 + lane * 16);
    f32x4 a0[2], a1[2];
#pragma unroll
    for (int i = 0; i <= 8; ++i) {
      if (i < 8) {
        const int T2 = i >> 2, P = i & 3;
        MF0(a0[i & 1], wo[P][0][0], of[T2 * 4]); MF0(a1[i & 1], wo[P][1][0], of[T2 * 4]);
        MF(a0[i & 1], wo[P][0][1], of[T2 * 4 + 1]); MF(a1[i & 1], wo[P][1][1], of[T2 * 4 + 1]);
      }
      if (i > 0) {
        const int j = i - 1;
        if (i == 8) asm volatile("s_nop 7" : "+v"(a0[j & 1]), "+v"(a1[j & 1])); else asm volatile("" : "+v"(a0[j & 1]), "+v"(a1[j & 1]));
        const f32x4 b0 = a0[j & 1], b1 = a1[j & 1];
        st128((u32x4){pk2(b0[0], b0[1]), pk2(b0[2], b0[3]), pk2(b1[0], b1[1]), pk2(b1[2], b1[3])}, rS, lane * 16, (s * NSLAB + h) * 32768 + (((j >> 2) * 4 + wave) * 4 + (j & 3)) * 1024);
      }
      if (i < 8) {
        const int T2 = i >> 2, P = i & 3;
        MF(a0[i & 1], wo[P][0][2], of[T2 * 4 + 2]); MF(a1[i & 1], wo[P][1][2], of[T2 * 4 + 2]);
        MF(a0[i & 1], wo[P][0][3], of[T2 * 4 + 3]); MF(a1[i & 1], wo[P][1][3], of[T2 * 4 + 3]);
      }
    }
    AS(4);
  }
  vmwait<0>();
  put_flag(rF, (F_AH + h) * 128 + wave * 4, NSEG, lane);
}

__device__ __forceinline__ void red_role(const Params& p, LAS char* lds, int r) {
  const int tid = threadIdx.x, lane = tid & 63, wave = __builtin_amdgcn_readfirstlane(tid >> 6);
  const int g = lane >> 4, c = lane & 15;
  char* ws = p.ws;
  const rsrc_t rF = mkrsrc(ws + OFF_FLG, FLG_BYTES);
  const rsrc_t rS = mkrsrc(ws + OFF_SL, NSEG * NSLAB * 32768);
  const int T = r >> 4, w = (r >> 2) & 3, P = r & 3;
  bf16x8 a0, a1;
#pragma unroll
  for (int j = 0; j < 8; ++j) { a0[j] = (__bf16)((8 * g + j == c) ? 1.f : 0.f); a1[j] = (__bf16)((8 * g + j == 16 + c) ? 1.f : 0.f); }
  const unsigned fA = lane < 8 ? (unsigned)((F_AH + wave * 8 + lane) * 128 + w * 4) : 0x80000000u;
  const int tok = 8 * (c >> 2) + 4 * T + (c & 3);
  int budget = SPIN_LIMIT;
  unsigned fl = 0;
  u32x4 v[10]; u32x2 xv = {0, 0};
  auto fetch = [&](int s) {
    if ((s & 7) == 0) {
      const unsigned fY = (lane < 8 && wave + 4 * (lane >> 2) < 7) ? (unsigned)((F_SP + (s >> 3) * 7 + wave + 4 * (lane >> 2)) * 128 + (lane & 3) * 4) : 0x80000000u;
      wait_flags(rF, fY, 1, budget);
    }
    const unsigned sb = s * NSLAB * 32768 + r * 1024;
    v[8] = ld128(rS, lane * 16, sb + (32 + wave) * 32768, 16);
    v[9] = ld128(rS, wave < 3 ? (unsigned)(lane * 16) : 0x80000000u, sb + (36 + (wave < 3 ? wave : 0)) * 32768, 16);
    if (wave < 2) xv = *(const u32x2*)(p.x + (size_t)(s * 32 + tok) * DM + 128 * w + 32 * P + 16 * wave + 4 * g);
    for (int spin = 0; __builtin_amdgcn_ballot_w64(lane < 8 && fl < (unsigned)(s + 1)) != 0 && spin < budget; ++spin) {
      if (spin) __builtin_amdgcn_s_sleep(PSL);
      asm volatile("" ::: "memory");
      fl = __builtin_amdgcn_raw_buffer_load_b32(rF, fA, 0, 16);
      if (spin == budget - 1) budget = 0;
    }
#pragma unroll
    for (int n = 0; n < 8; ++n) v[n] = ld128(rS, lane * 16, sb + (wave * 8 + n) * 32768, 16);
  };
  fetch(0);
  for (int s = 0; s < NSEG; ++s) {
    f32x4 d0 = {}, d1 = {};
#pragma unroll
    for (int n = 9; n >= 0; --n) { const int q = n >= 8 ? n : 7 - n; d0 = mfma16(a0, asb8(v[q]), d0); d1 = mfma16(a1, asb8(v[q]), d1); }
    const u32x2 xc = xv;
    LAS char* b = lds + (s & 1) * 8192;
    *(LAS f32x4*)(b + (wave * 2) * 1024 + lane * 16) = d0;
    *(LAS f32x4*)(b + (wave * 2 + 1) * 1024 + lane * 16) = d1;
    const bool ahead = ((s + 1) & 7) != 0 && __builtin_amdgcn_ballot_w64(lane < 8 && fl < (unsigned)(s + 2)) == 0;
    if (ahead) fetch(s + 1);
    lds_barrier();
    if (wave < 2) {
      f32x4 t = *(LAS f32x4*)(b + wave * 1024 + lane * 16);
#pragma unroll
      for (int n = 1; n < 4; ++n) t += *(LAS f32x4*)(b + (n * 2 + wave) * 1024 + lane * 16);
      *(u32x2*)(p.out + (size_t)(s * 32 + tok) * DM + 128 * w + 32 * P + 16 * wave + 4 * g) =
          (u32x2){pk2(t[0] + bflo(xc.x), t[1] + bfhi(xc.x)), pk2(t[2] + bflo(xc.y), t[3] + bfhi(xc.y))};
    }
    if (!ahead && s + 1 < NSEG) fetch(s + 1);
  }
}

__device__ __forceinline__ float rowsum16(float x) {
  x += __builtin_bit_cast(float, __builtin_amdgcn_update_dpp(0, __builtin_bit_cast(int, x), 0x128, 0xf, 0xf, false));
  x += __builtin_bit_cast(float, __builtin_amdgcn_update_dpp(0, __builtin_bit_cast(int, x), 0x124, 0xf, 0xf, false));
  x += __builtin_bit_cast(float, __builtin_amdgcn_update_dpp(0, __builtin_bit_cast(int, x), 0x122, 0xf, 0xf, false));
  x += __builtin_bit_cast(float, __builtin_amdgcn_update_dpp(0, __builtin_bit_cast(int, x), 0x121, 0xf, 0xf, false));
  return x;
}
__device__ __forceinline__ void sp_role(const Params& p, LAS char* lds, int tile) {
  const int tid = threadIdx.x, lane = tid & 63, wave = __builtin_amdgcn_readfirstlane(tid >> 6);
  const int g = lane >> 4, c = lane & 15;
  char* ws = p.ws;
  const unsigned lbase = (unsigned)(uintptr_t)lds;
  const rsrc_t rF = mkrsrc(ws + OFF_FLG, FLG_BYTES);
  int budget = SPIN_LIMIT;
  proj_role<3, 1>(p, lds, tile % 14, (tile / 14) * 16, (tile / 14) * 16 + 16, F_Z2 + tile);
  for (int s = tile; s < NSEG; s += 56) {
    const rsrc_t rZ = mkrsrc(ws + OFF_Z2 + (size_t)s * 57344, 57344);
    const rsrc_t rN = mkrsrc(ws + OFF_ZN + (size_t)s * 57344, 57344);
    wait_flags(rF, lane < 14 ? (unsigned)((F_Z2 + (s >> 4) * 14 + ((wave * 14 + lane) >> 2)) * 128 + ((wave * 14 + lane) & 3) * 4) : 0x80000000u, s + 1, budget);
    u32x4 z[14];
#pragma unroll
    for (int n = 0; n < 14; ++n) z[n] = ld128(rZ, lane * 16, (wave * 14 + n) * 1024, 16);
    float f[14][8], sum[8] = {};
#pragma unroll
    for (int n = 0; n < 14; ++n) {
      f[n][0] = bflo(z[n].x); f[n][1] = bfhi(z[n].x); f[n][2] = bflo(z[n].y); f[n][3] = bfhi(z[n].y);
      f[n][4] = bflo(z[n].z); f[n][5] = bfhi(z[n].z); f[n][6] = bflo(z[n].w); f[n][7] = bfhi(z[n].w);
#pragma unroll
      for (int j = 0; j < 8; ++j) sum[j] += f[n][j];
    }
    LAS float* red = (LAS float*)lds;
#pragma unroll
    for (int j = 0; j < 8; ++j) { const float t = rowsum16(sum[j]); if (c == 0) red[wave * 32 + g * 8 + j] = t; }
    lds_barrier();
    float q[8] = {};
#pragma unroll
    for (int j = 0; j < 8; ++j) {
      const float mean = (red[g * 8 + j] + red[32 + g * 8 + j] + red[64 + g * 8 + j] + red[96 + g * 8 + j]) * (1.f / FH);
#pragma unroll
      for (int n = 0; n < 14; ++n) { f[n][j] -= mean; q[j] += f[n][j] * f[n][j]; }
    }
#pragma unroll
    for (int j = 0; j < 8; ++j) { const float t = rowsum16(q[j]); if (c == 0) red[128 + wave * 32 + g * 8 + j] = t; }
    lds_barrier();
    float rs[8];
#pragma unroll
    for (int j = 0; j < 8; ++j)
      rs[j] = 1.f / sqrtf((red[128 + g * 8 + j] + red[160 + g * 8 + j] + red[192 + g * 8 + j] + red[224 + g * 8 + j]) * (1.f / FH) + 1e-5f);
#pragma unroll
    for (int n = 0; n < 14; ++n)
      st128((u32x4){pk2(f[n][0] * rs[0], f[n][1] * rs[1]), pk2(f[n][2] * rs[2], f[n][3] * rs[3]), pk2(f[n][4] * rs[4], f[n][5] * rs[5]), pk2(f[n][6] * rs[6], f[n][7] * rs[7])},
            rN, lane * 16, (wave * 14 + n) * 1024);
    vmwait<0>();
    lds_barrier();
    put_flag(rF, (F_LN + s) * 128, 1, tid);
  }
  const int tp = tile / 7, fc = tile % 7;
  if (wave == 0) {
    wait_flags(rF, (unsigned)((F_LN + lane) * 128), 1, budget);
    __builtin_amdgcn_fence(__ATOMIC_ACQUIRE, "agent");
    vmwait<0>();
  }
  lds_barrier();
  const char* zn = ws + OFF_ZN;
  unsigned boff[4];
#pragma unroll
  for (int Tt = 0; Tt < 4; ++Tt) boff[Tt] = (unsigned)(((Tt >> 1) * 32 + 8 * (c >> 2) + 4 * (Tt & 1) + (c & 3)) * S_ + 8 * g) * 2;
  const char* wsp = (const char*)(p.w_sp + (size_t)(tp * 256 + wave * 64) * S_);
  const unsigned l16 = lane * 16;
  auto piece = [&](int kk, int n) {
    const unsigned dst = lbase + (kk % 3) * 49152;
    if (n < 4) {
      const char* sb = zn + ((size_t)(kk * 2 + (wave >> 1)) * 56 + fc * 8 + (wave & 1) * 4) * 1024;
      const unsigned m0v = dst + wave * 4096;
      if (n == 0) glds_s<0>(l16, sb, m0v); else if (n == 1) glds_s<1024>(l16, sb, m0v); else if (n == 2) glds_s<2048>(l16, sb, m0v); else glds_s<3072>(l16, sb, m0v);
    } else {
      const int q = n - 4;
      glds_s<0>(boff[q >> 1], wsp + kk * 128 + (q & 1) * 64, dst + 16384 + (wave * 8 + (q & 1) * 4 + (q >> 1)) * 1024);
    }
  };
  f32x4 acc[4][8] = {};
#pragma unroll
  for (int n = 0; n < 12; ++n) piece(0, n);
#pragma unroll
  for (int n = 0; n < 12; ++n) piece(1, n);
  for (int kk = 0; kk < 32; ++kk) {
    if (kk + 1 < 32) vmwait<12>(); else vmwait<0>();
    lds_barrier();
    LAS char* b = lds + (kk % 3) * 49152 + lane * 16;
    bf16x8 bf[2][4], af[2][4];
#pragma unroll
    for (int Tt = 0; Tt < 4; ++Tt) bf[0][Tt] = *(LAS bf16x8*)(b + 16384 + (wave * 8 + Tt) * 1024);
#pragma unroll
    for (int q = 0; q < 4; ++q) af[0][q] = *(LAS bf16x8*)(b + q * 1024);
#pragma unroll
    for (int ch = 0; ch < 4; ++ch) {
      SB();
      if (ch < 3) {
#pragma unroll
        for (int q = 0; q < 4; ++q) af[(ch + 1) & 1][q] = *(LAS bf16x8*)(b + ((ch + 1) * 4 + q) * 1024);
      }
      if (ch == 0) {
#pragma unroll
        for (int Tt = 0; Tt < 4; ++Tt) bf[1][Tt] = *(LAS bf16x8*)(b + 16384 + (wave * 8 + 4 + Tt) * 1024);
      }
#pragma unroll
      for (int q = 0; q < 4; ++q) {
#pragma unroll
        for (int Tt = 0; Tt < 4; ++Tt) acc[Tt][(ch & 1) * 4 + q] = mfma16(af[ch & 1][q], bf[ch >> 1][Tt], acc[Tt][(ch & 1) * 4 + q]);
        if (q < 3) { SB(); if (kk + 2 < 32) piece(kk + 2, ch * 3 + q); SB(); }
      }
    }
    SB();
  }
  lds_barrier();
#pragma unroll 4
  for (int n = 0; n < 32; ++n) {
    const int e = wave * 32 + n;
    const int G = ((lane >> 2) - (e & 3)) & 15;
    glds16(p.w_out + (size_t)(fc * 128 + e) * DM + G * 32 + (lane & 3) * 8, lbase + e * 1024);
  }
  {
    const int rg = MAPV ? tp / 2 : tp / 3;
    wait_flags(rF, lane < 4 ? (unsigned)((F_Z1 + rg * 7 + fc) * 128 + lane * 4) : 0x80000000u, tp * 8 + 8, budget);
  }
  const rsrc_t rZ1 = mkrsrc(ws + OFF_Z1, NSEG * 2 * 28 * 1024);
  bf16x8 ub[4][4];
#pragma unroll
  for (int Tt = 0; Tt < 4; ++Tt) {
    const int sg = tp * 8 + wave * 2 + (Tt >> 1);
    const float bias = bflo((unsigned)p.b_sp[sg * 32 + 8 * (c >> 2) + 4 * (Tt & 1) + (c & 3)]);
#pragma unroll
    for (int m = 0; m < 4; ++m) {
      const u32x4 z = ld128(rZ1, lane * 16, ((sg * 2 + (Tt & 1)) * 28 + fc * 4 + m) * 1024, 16);
      const f32x4 g0 = acc[Tt][2 * m] + bias, g1 = acc[Tt][2 * m + 1] + bias;
      ub[Tt][m] = asb8((u32x4){pk2(bflo(z.x) * g0[0], bflo(z.y) * g0[1]), pk2(bflo(z.z) * g0[2], bflo(z.w) * g0[3]),
                               pk2(bfhi(z.x) * g1[0], bfhi(z.y) * g1[1]), pk2(bfhi(z.z) * g1[2], bfhi(z.w) * g1[3])});
    }
  }
  vmwait<0>();
  lds_barrier();
  const rsrc_t rS = mkrsrc(ws + OFF_SL, NSEG * NSLAB * 32768);
  for (int P16 = 0; P16 < 16; ++P16) {
    bf16x8 wf[2][4];
#pragma unroll
    for (int t = 0; t < 2; ++t)
#pragma unroll
      for (int m = 0; m < 4; ++m) {
        bf16x4 pc[2];
#pragma unroll
        for (int u = 0; u < 2; ++u) {
          const int e = 32 * m + 16 * u + 4 * g + (c >> 2);
          pc[u] = trrd(lds + e * 1024 + ((P16 + (e & 3)) & 15) * 64 + (c & 3) * 16 + t * 8);
        }
        wf[t][m] = cat8(pc[0], pc[1]);
      }
#pragma unroll
    for (int Tt = 0; Tt < 4; ++Tt) {
      f32x4 a0 = {}, a1 = {};
#pragma unroll
      for (int m = 0; m < 4; ++m) { a0 = mfma16(wf[0][m], ub[Tt][m], a0); a1 = mfma16(wf[1][m], ub[Tt][m], a1); }
      const int sg = tp * 8 + wave * 2 + (Tt >> 1);
      st128((u32x4){pk2(a0[0], a0[1]), pk2(a0[2], a0[3]), pk2(a1[0], a1[1]), pk2(a1[2], a1[3])}, rS, lane * 16, (sg * NSLAB + 32 + fc) * 32768 + ((Tt & 1) * 16 + P16) * 1024);
    }
  }
  vmwait<0>();
  put_flag(rF, (F_SP + tile) * 128 + wave * 4, 1, lane);
}

constexpr int L_MST = 0, L_KX = 8192, L_VX = 16384, L_QI = 24576, L_OI = 30976, L_WO = 32768, L_S1 = 98304, L_WV = 98304;
constexpr int QI_G = 400, QI_W = 4 * QI_G;
#define ST(i)
#define CNT(i)
#define TRS(x)
#define TA 300
#define TB 350
#define D1 0
#define D2 0

__device__ __forceinline__ unsigned min3u(unsigned a, unsigned b, unsigned c) { return min(min(a, b), c); }
__device__ __forceinline__ unsigned min4u(u32x4 a) { return min(min3u(a.x, a.y, a.z), a.w); }

__device__ __forceinline__ void chain_role(const Params& p, LAS char* lds, int h, int a) {
  const int tid = threadIdx.x, lane = tid & 63, wave = __builtin_amdgcn_readfirstlane(tid >> 6);
  const int g = lane >> 4, c = lane & 15;
  char* ws = p.ws;
  const unsigned lbase = (unsigned)(uintptr_t)lds;
  const unsigned sel = __builtin_amdgcn_readfirstlane(((const unsigned*)(ws + OFF_FLG))[FLG_SEL]) & 1u;

  bf16x16 wq[2][8], wk[2][8], wv[2][8];
  {
    auto issue = [&](const u16* W, int half, int soff) {
#pragma unroll 4
      for (int n = 0; n < 16; ++n) {
        const int row = (wave * 16 + n) * 4 + (lane >> 4);
        const int x = lane & 15;
        const int rot = (row & 3) + 4 * ((row >> 4) & 1);
        const int G = ((x >> 1) - rot) & 7;
        glds16(W + (size_t)(half * 256 + row) * (NH * DH) + h * DH + G * 16 + (x & 1) * 8, lbase + soff + (wave * 16 + n) * 1024);
      }
    };
    auto fetch = [&](bf16x16 (&w)[2][8], int half, int soff) {
#pragma unroll
      for (int m = 0; m < 4; ++m) {
        const int s4 = (wave + m) & 3;
#pragma unroll
        for (int t = 0; t < 2; ++t) {
          bf16x4 pc[4];
#pragma unroll
          for (int u = 0; u < 4; ++u) {
            const int q = c >> 2;
            const int row = 64 * s4 + 32 * (u & 1) + 16 * (q & 1) + 8 * (u >> 1) + 4 * (g >> 1) + 2 * (q >> 1) + (g & 1);
            const int rot = (row & 3) + 4 * ((row >> 4) & 1);
            pc[u] = trrd(lds + soff + row * 256 + ((wave * 2 + t + rot) & 7) * 32 + (c & 3) * 8);
          }
          w[t][2 * m + half] = cat16(cat8(pc[0], pc[1]), cat8(pc[2], pc[3]));
        }
      }
    };
    issue(p.Wq, 0, 0); issue(p.Wq, 1, L_S1);
    asm volatile("s_waitcnt vmcnt(16)" ::: "memory"); lds_barrier();
    fetch(wq, 0, 0); lds_barrier();
    issue(p.Wk, 0, 0);
    asm volatile("s_waitcnt vmcnt(16)" ::: "memory"); lds_barrier();
    fetch(wq, 1, L_S1); lds_barrier();
    issue(p.Wk, 1, L_S1);
    asm volatile("s_waitcnt vmcnt(16)" ::: "memory"); lds_barrier();
    fetch(wk, 0, 0); lds_barrier();
    issue(p.Wv, 0, 0);
    asm volatile("s_waitcnt vmcnt(16)" ::: "memory"); lds_barrier();
    fetch(wk, 1, L_S1); lds_barrier();
    issue(p.Wv, 1, L_S1);
    asm volatile("s_waitcnt vmcnt(16)" ::: "memory"); lds_barrier();
    fetch(wv, 0, 0); lds_barrier();
#pragma unroll 4
    for (int n = 0; n < 16; ++n) {
      const int e = (wave * 16 + n) * 2 + (lane >> 5);
      const int x = lane & 31;
      const int rot = (e & 1) + 2 * ((e >> 2) & 1) + 4 * ((e >> 3) & 1);
      const int G = ((x >> 1) - rot) & 15;
      glds16(p.Wo + (size_t)(h * DH + e) * DM + a * 256 + G * 16 + (x & 1) * 8, lbase + L_WO + (wave * 16 + n) * 1024);
    }
    asm volatile("s_waitcnt vmcnt(16)" ::: "memory"); lds_barrier();
    fetch(wv, 1, L_S1);
    asm volatile("s_waitcnt vmcnt(0)" ::: "memory"); lds_barrier();
    {
      bf16x8 wlo[8], whi[8];
#pragma unroll
      for (int n = 0; n < 8; ++n) {
        const int q = c >> 2, rot = (q & 1) + 2 * (g & 1) + 4 * (q >> 1);
        LAS char* b = lds + L_WO + (64 * (n >> 2) + 16 * (g >> 1) + 8 * (q >> 1) + 4 * (g & 1) + (q & 1)) * 512 + ((wave * 4 + (n & 3) + rot) & 15) * 32 + (c & 3) * 8;
        wlo[n] = cat8(trrd(b), trrd(b + 2 * 512)); whi[n] = cat8(trrd(b + 32 * 512), trrd(b + 34 * 512));
      }
      lds_barrier();
#pragma unroll
      for (int n = 0; n < 8; ++n) { LAS char* d = lds + L_WO + (wave * 8 + n) * 2048 + lane * 16; *(LAS bf16x8*)d = wlo[n]; *(LAS bf16x8*)(d + 1024) = whi[n]; }
    }
#pragma unroll
    for (int t = 0; t < 2; ++t)
#pragma unroll
      for (int u = 5; u < 8; ++u) {
        LAS char* d = lds + L_WV + (wave * 6 + (u == 5 ? 4 : (u - 6) * 2) + t) * 2048 + lane * 16;
        *(LAS bf16x8*)d = __builtin_shufflevector(wv[t][u], wv[t][u], 0, 1, 2, 3, 4, 5, 6, 7);
        *(LAS bf16x8*)(d + 1024) = __builtin_shufflevector(wv[t][u], wv[t][u], 8, 9, 10, 11, 12, 13, 14, 15);
      }
#pragma unroll
    for (int t = 0; t < 2; ++t)
#pragma unroll
      for (int u = 0; u < 8; ++u) {
        asm volatile("" : "+a"(wk[t][u]));
        if (u < 5) asm volatile("" : "+a"(wv[t][u])); else asm volatile("" : "+a"(wq[t][u]));
      }
  }

  const rsrc_t rX = mkrsrc(ws + OFF_X, (2u << 20) + FLG_BYTES);
  const rsrc_t rK = mkrsrc(ws + OFF_KF + (size_t)h * NSEG * 8192, NSEG * 8192);
  const rsrc_t rV = mkrsrc(ws + OFF_VF + (size_t)h * NSEG * 8192, NSEG * 8192);
  const rsrc_t rKVM = mkrsrc(ws + (a ? OFF_VM : OFF_KM) + (size_t)h * NSEG * 2048, NSEG * 2048);
  const unsigned kvd = a ? 256u : 16u;
  const int i16 = c, pr = (i16 >> 1) & 1;
  const int sidx = pr ? 0xEEEE : 0x4444;
  const unsigned vM = X_M + sel * 8192 + wave * 1024 + (i16 >> 2) * 256 + (g >> 1) * 128 + (i16 & 1) * 64 + (g & 1) * 32 + pr * 16;
  const int myline = ((a * 4 + wave) * 4 + g) * 2 + (c >> 3);
  const unsigned vPst = (myline >> 1) * 16384 + sel * 8192 + (myline & 1) * 4096 + h * 128 + (c & 7) * 16;
  const int rline = h * 2 + a;
  const unsigned vPld = (rline >> 1) * 16384 + sel * 8192 + (rline & 1) * 4096 + lane * 16;
  const unsigned vMst = ((g < 2) && (c < 8)) ? (unsigned)(X_M + sel * 8192 + rline * 128 + g * 64 + c * 8) : 0x80000000u;
  const bool lo8 = c < 8;
  const float CEXP = 0.08838834764831845f * 1.4426950408889634f;
  int spin_limit = SPIN_LIMIT;
  const unsigned vKV0 = a ? (unsigned)((2 * wave) * 256 + lane * 4) : (unsigned)(wave * 512 + (c >> 2) * 128 + (2 * g) * 16 + (c & 3) * 4);
  int qi_st = L_QI + wave * QI_W + (c >> 2) * QI_G + (2 * g) * 16 + (c & 3) * 4;
  asm volatile("" : "+v"(qi_st));
  int qf_ld = L_QI + g * QI_G + (c & 7) * 16;
  asm volatile("" : "+v"(qf_ld));
  const int kf_ld = L_QI + g * QI_G + 256 + (2 * (c >> 2) + (c & 1)) * 16;
  int wo_ld = L_WO + wave * 16384 + lane * 16, wv_ld = L_WV + wave * 12288 + lane * 16;
  asm volatile("" : "+v"(wo_ld), "+v"(wv_ld));
  const int oi_st = L_MST + (wave >> 1) * 1024 + ((2 * (wave & 1) + (c >> 3)) * 16 + 4 * ((c & 7) >> 1) + 2 * (g >> 1) + (c & 1)) * 16 + (g & 1) * 8;

  u32x4 pf[4];
  auto kv_issue = [&](int s) {
    pf[0] = ld128(rK, lane * 16, s * 8192 + wave * 2048, 16); pf[1] = ld128(rK, lane * 16, s * 8192 + wave * 2048 + 1024, 16);
    pf[2] = ld128(rV, lane * 16, s * 8192 + wave * 2048, 16); pf[3] = ld128(rV, lane * 16, s * 8192 + wave * 2048 + 1024, 16);
  };
  auto kv_commit = [&]() {
    LAS char* d = lds + wave * 2048 + lane * 16;
    *(LAS u32x4*)(d + L_KX) = pf[0]; *(LAS u32x4*)(d + L_KX + 1024) = pf[1]; *(LAS u32x4*)(d + L_VX) = pf[2]; *(LAS u32x4*)(d + L_VX + 1024) = pf[3];
  };
  wait_flags(rX, lane < 4 ? (unsigned)(OFF_FLG + (F_KV + h) * 128 + lane * 4) : 0x80000000u, 2, spin_limit);
  kv_issue(0); kv_commit();
  int kvknown = 2;
  int mst_st = L_MST + wave * 1024 + lane * 16, vx_ld = L_VX + wave * 2048 + lane * 16;
  asm volatile("" : "+v"(mst_st), "+v"(vx_ld));
  const unsigned vTS = wave == 3 ? (unsigned)(X_TS + sel * 8192 + lane * 128) : 0x80000000u;
  const unsigned vTSst = (lane == 0 && wave == 3) ? (unsigned)(X_TS + sel * 8192 + rline * 128) : 0x80000000u;
  const unsigned vPz = vPld + wave * 1024;
  const unsigned vCH = (tid == 64) ? (unsigned)(OFF_FLG + (F_CH + rline) * 128) : 0x80000000u;
  const unsigned vMz = wave == 3 ? vMst : 0x80000000u;
  if (wave == 3) {
    bf16x8 asel;
#pragma unroll
    for (int j = 0; j < 8; ++j) asel[j] = (__bf16)((c == j) ? 1.0f : 0.0f);
    *(LAS bf16x8*)(lds + L_OI + lane * 16) = asel;
  }
  int mst_ld[3];
#pragma unroll
  for (int m = 1; m < 4; ++m) { mst_ld[m - 1] = L_MST + ((wave + m) & 3) * 1024 + lane * 16; asm volatile("" : "+v"(mst_ld[m - 1])); }

  f32x4 aq0 = zero4(), aq1 = zero4(), ak0 = zero4(), ak1 = zero4(), av0 = zero4(), av1 = zero4();
  lds_barrier();
  bf16x8 kxf[8];
#pragma unroll
  for (int n = 0; n < 8; ++n) kxf[n] = *(LAS bf16x8*)(lds + L_KX + n * 1024 + lane * 16);
  u32x4 f0 = ld128(rX, vM, 0, 16), f1 = ld128(rX, vM, 4096, 16);
#define FENCE() asm volatile("" ::: "memory")
#define KEEP(x) asm volatile("" : "+v"(x))
#define Q0(u) do { if ((u) < 5) SMF_V(aq0, af[u], wq[0][u]); else SMF_A(aq0, af[u], wq[0][u]); } while (0)
#define Q1(u) do { if ((u) < 5) SMF_V(aq1, af[u], wq[1][u]); else SMF_A(aq1, af[u], wq[1][u]); } while (0)
#define QQ(u) do { Q0(u); Q1(u); } while (0)
#define K0(u) SMF_A(ak0, af[u], wk[0][u])
#define K1(u) SMF_A(ak1, af[u], wk[1][u])
#define KK(u) do { K0(u); K1(u); } while (0)
#define V0(u) SMF_A(av0, af[u], wv[0][u])
#define V1(u) SMF_A(av1, af[u], wv[1][u])
#define VV(u) do { V0(u); V1(u); } while (0)
#define LDA(u) do { FENCE(); af[u] = *(LAS bf16x8*)(lds + mst_ld[((u) >> 1) - 1] + ((u) & 1) * 4096); FENCE(); } while (0)
#define LDF(base, n) cat16(*(LAS bf16x8*)(lds + (base) + (n) * 2048), *(LAS bf16x8*)(lds + (base) + (n) * 2048 + 1024))
#define DPPMAX(x, ctl, rm) do { x = max(x, (int)__builtin_amdgcn_update_dpp((int)0x80000000, x, ctl, rm, 0xf, false)); KEEP(x); } while (0)
  for (int s = 0; s < NSEG; ++s) {
    const unsigned sM = (s & 3) * 16384;
    if (__builtin_expect(__builtin_amdgcn_ballot_w64(min(min4u(f0), min4u(f1)) == 0) != 0, 0)) {
      for (int spin = 0; spin < spin_limit; ++spin) {
        asm volatile("" ::: "memory");
        f0 = ld128(rX, vM, sM, 16); f1 = ld128(rX, vM, sM + 4096, 16); CNT(10);
        if (__builtin_amdgcn_ballot_w64(min(min4u(f0), min4u(f1)) == 0) == 0) break;
        if (spin == spin_limit - 1) spin_limit = 0;
      }
      asm volatile("s_nop 1" : "+v"(f0), "+v"(f1));
    }
    ST(0);
    bf16x8 af[8];
    af[0] = asb8(f0); af[1] = asb8(f1);
    *(LAS u32x4*)(lds + mst_st) = f0;
    *(LAS u32x4*)(lds + mst_st + 4096) = f1;
    FENCE();
    Q0(0);
    __builtin_amdgcn_raw_buffer_store_b128(__builtin_bit_cast(u32x4, av0), rX, vPz, ((s + 1) & 1) * 524288, 16);
    FENCE();
    Q1(0);
    __builtin_amdgcn_raw_buffer_store_b64(__builtin_bit_cast(u32x2, __builtin_shufflevector(av0, av0, 0, 1)), rX, vMz, ((s + 2) & 3) * 16384, 16);
    FENCE();
    Q0(1);
    __builtin_amdgcn_raw_buffer_store_b32((unsigned)s, rX, vCH, 0, 16);
    FENCE();
    Q1(1);
    lds_barrier();
    unsigned long long rt64;
    asm volatile("s_memrealtime %0" : "=s"(rt64) :: "memory");
    ST(1);
    bf16x16 wl[6];
    LDA(2); LDA(3); K0(0); LDA(4); LDA(5); K1(0); LDA(6); LDA(7); K0(1); K1(1);
    VV(0); V0(1);
    asm volatile("s_waitcnt lgkmcnt(0)" : "+s"(rt64) :: "memory");
    const unsigned rown = (unsigned)rt64;
    __builtin_amdgcn_raw_buffer_store_b32(rown, rX, vTSst, (s & 3) * 16384, 16);
    FENCE();
    V1(1);
    QQ(2); FENCE(); wl[0] = LDF(wv_ld, 0); FENCE();
    QQ(3); FENCE(); wl[1] = LDF(wv_ld, 1); FENCE();
    QQ(4); FENCE(); wl[2] = LDF(wv_ld, 2); FENCE();
    QQ(5); FENCE(); wl[3] = LDF(wv_ld, 3); FENCE();
    QQ(6); QQ(7);
    K0(2); K1(2); K0(3);
    asm volatile("s_nop 0" : "+v"(aq0), "+v"(aq1));
    {
      float t0 = aq0[0] + aq0[2], t1 = aq1[0] + aq1[2]; KEEP(t0); KEEP(t1);
      K1(3);
      float t2 = aq0[1] + aq0[3], t3 = aq1[1] + aq1[3]; KEEP(t2); KEEP(t3);
      K0(4);
      const unsigned d0 = pk2(t0, t1), d1 = pk2(t2, t3);
      *(LAS unsigned*)(lds + qi_st) = d0; *(LAS unsigned*)(lds + qi_st + 16) = d1;
      FENCE();
    }
    K1(4); K0(5); K1(5); K0(6);
    lds_barrier();
    TRS(tb2);
    ST(2);
    bf16x8 qf[4], kf[4];
    qf[0] = *(LAS bf16x8*)(lds + qf_ld); qf[1] = *(LAS bf16x8*)(lds + qf_ld + QI_W); FENCE();
    K1(6); FENCE();
    qf[2] = *(LAS bf16x8*)(lds + qf_ld + 2 * QI_W); qf[3] = *(LAS bf16x8*)(lds + qf_ld + 3 * QI_W); FENCE();
    K0(7); FENCE(); wl[4] = LDF(wv_ld, 4); FENCE();
    K1(7); FENCE(); wl[5] = LDF(wv_ld, 5); FENCE();
    V0(2); V1(2); V0(3);
    asm volatile("" : "+v"(ak0), "+v"(ak1));
    unsigned kd0, kd1;
    f32x4 sx0, sx1, sm;
    {
      float t0 = ak0[0] + ak0[2], t1 = ak1[0] + ak1[2]; KEEP(t0); KEEP(t1);
      V1(3);
      float t2 = ak0[1] + ak0[3], t3 = ak1[1] + ak1[3]; KEEP(t2); KEEP(t3);
      MF0m(sx0, kxf[0], qf[0]);
      kd0 = pk2(t0, t1); kd1 = pk2(t2, t3);
      *(LAS unsigned*)(lds + qi_st + 256) = kd0; *(LAS unsigned*)(lds + qi_st + 256 + 16) = kd1;
    }
    MF0m(sx1, kxf[1], qf[0]);
    MFm(sx0, kxf[2], qf[1]);
    int ts = (int)__builtin_amdgcn_raw_buffer_load_b32(rX, vTS, (s & 3) * 16384, 16);
    MFm(sx1, kxf[3], qf[1]);
    MFm(sx0, kxf[4], qf[2]); MFm(sx1, kxf[5], qf[2]);
    lds_barrier();
    TRS(tb3);
    ST(3);
    kf[0] = *(LAS bf16x8*)(lds + kf_ld); kf[1] = *(LAS bf16x8*)(lds + kf_ld + QI_W);
    MFm(sx0, kxf[6], qf[3]);
    kf[2] = *(LAS bf16x8*)(lds + kf_ld + 2 * QI_W); kf[3] = *(LAS bf16x8*)(lds + kf_ld + 3 * QI_W);
    MFm(sx1, kxf[7], qf[3]);
    const bf16x8 vx0 = *(LAS bf16x8*)(lds + vx_ld), vx1 = *(LAS bf16x8*)(lds + vx_ld + 1024);
    FENCE();
    bf16x16 wo[8];
    SMF_V(av0, af[6], wl[0]); SMF_V(av1, af[6], wl[1]); FENCE();
    wo[0] = LDF(wo_ld, 0);
    MF0m(sm, kf[0], qf[0]);
    wo[1] = LDF(wo_ld, 1);
    MFm(sm, kf[1], qf[1]);
    float mm = max3f(sx0[0], sx0[1], sx0[2]), m2 = max3f(sx0[3], sx1[0], sx1[1]); KEEP(mm); KEEP(m2);
    MFm(sm, kf[2], qf[2]);
    m2 = max3f(m2, sx1[2], sx1[3]); mm = max2f(mm, m2); KEEP(mm);
    MFm(sm, kf[3], qf[3]);
    u32x2 on_lo, on_hi;
    asm volatile("v_mov_b64 %0, %2\n\tv_mov_b64 %1, %2" : "=v"(on_lo), "=v"(on_hi) : "s"(0x3f803f803f803f80ull));
    const bf16x8 ones = asb8(__builtin_shufflevector(on_lo, on_hi, 0, 1, 2, 3));
    wo[4] = LDF(wo_ld, 4); FENCE();
    SMF_V(av0, af[7], wl[2]); FENCE(); wo[5] = LDF(wo_ld, 5); FENCE(); SMF_V(av1, af[7], wl[3]);
    SMF_V(av0, af[5], wl[4]);
    asm volatile("" : "+v"(sm));
    mm = max3f(mm, sm[0], sm[1]);
    { float t_;
      asm volatile("v_mov_b32 %1, %0\n\tv_smfmac_f32_16x16x64_bf16 %2, %4, %5, %8\n\ts_nop 0\n\tv_permlane32_swap_b32 %0, %1\n\tv_max_f32 %0, %0, %1\n\tv_mov_b32 %1, %0\n\t"
                   "v_smfmac_f32_16x16x64_bf16 %3, %6, %7, %8\n\ts_nop 0\n\tv_permlane16_swap_b32 %0, %1\n\tv_max_f32 %0, %0, %1"
                   : "+v"(mm), "=&v"(t_), "+v"(av1), "+v"(av0) : "v"(af[5]), "v"(wl[5]), "v"(af[4]), "a"(wv[0][4]), "v"(sidx)); }
    V1(4);
    const float mc = mm * CEXP;
    float ex[8];
#pragma unroll
    for (int j = 0; j < 4; ++j) { ex[j] = __builtin_amdgcn_exp2f(sx0[j] * CEXP - mc); ex[4 + j] = __builtin_amdgcn_exp2f(sx1[j] * CEXP - mc); }
    const u32x4 exp_ = {pk2(ex[0], ex[1]), pk2(ex[2], ex[3]), pk2(ex[4], ex[5]), pk2(ex[6], ex[7])};
    f32x4 ax, dn;
    u32x2 vv;
    { const bf16x8 b0 = asb8((u32x4){lo8 ? exp_.x : 0u, lo8 ? exp_.y : 0u, lo8 ? exp_.z : 0u, lo8 ? exp_.w : 0u});
      const bf16x8 ea = asb8(exp_);
      MFN0(ax, vx0, b0);
      float e0 = __builtin_amdgcn_exp2f(sm[0] * CEXP - mc); KEEP(e0);
      MF0(dn, ones, ea);
      float e1 = __builtin_amdgcn_exp2f(sm[1] * CEXP - mc); KEEP(e1);
      const bf16x8 b1 = asb8((u32x4){lo8 ? 0u : exp_.x, lo8 ? 0u : exp_.y, lo8 ? 0u : exp_.z, lo8 ? 0u : exp_.w});
      MFN(ax, vx1, b1);
      asm volatile("" : "+v"(av0), "+v"(av1));
      const unsigned em = pk2(e0, e1);
      vv = (u32x2){pk2(av0[0] + av0[2], av0[1] + av0[3]), pk2(av1[0] + av1[2], av1[1] + av1[3])};
      const u32x2 bm = {lo8 ? em : 0u, lo8 ? 0u : em};
      const bf16x4 on2 = __builtin_shufflevector(ones, ones, 0, 1, 2, 3);
      asm volatile("s_nop 1\n\tv_mfma_f32_16x16x16_bf16 %0, %2, %4, %0\n\tv_mfma_f32_16x16x16_bf16 %1, %3, %4, %1" : "+v"(dn), "+v"(ax) : "v"(on2), "v"(vv), "v"(bm) : "memory"); }
    f32x4 ap0 = zero4(), ap1 = zero4(), ap2 = zero4();
    asm volatile("s_nop 0" : "+v"(dn), "+v"(ax) :: "memory");
    const float rinv = __builtin_amdgcn_rcpf(dn[0]);
    {
      const u32x2 o = {pk2(ax[0] * rinv, ax[1] * rinv), pk2(ax[2] * rinv, ax[3] * rinv)};
      *(LAS u32x2*)(lds + oi_st) = o;
    }
    FENCE();
    asm volatile("v_subrev_u32 %0, %1, %0" : "+v"(ts) : "s"(rown) : "memory");
    f32x4 ap3 = zero4();
    __builtin_amdgcn_raw_buffer_store_b32(a ? vv.x : kd0, rKVM, vKV0, s * 2048, 16);
    __builtin_amdgcn_raw_buffer_store_b32(a ? vv.y : kd1, rKVM, vKV0, s * 2048 + kvd, 16);
    if (s == NSEG - 1) break;
    lds_barrier();
    TRS(tb4);
    ST(4);
    const bf16x8 o0 = *(LAS bf16x8*)(lds + L_MST + lane * 16), o1 = *(LAS bf16x8*)(lds + L_MST + 1024 + lane * 16);
    FENCE();
    wo[2] = LDF(wo_ld, 2); wo[3] = LDF(wo_ld, 3); wo[6] = LDF(wo_ld, 6); wo[7] = LDF(wo_ld, 7);
    FENCE();
    kv_issue(s + 1);
    FENCE();
    DPPMAX(ts, 0x111, 0xf); DPPMAX(ts, 0x112, 0xf);
    SMF_V(ap0, o0, wo[0]); DPPMAX(ts, 0x114, 0xf); SMF_V(ap1, o0, wo[1]); DPPMAX(ts, 0x118, 0xf); SMF_V(ap0, o1, wo[4]); DPPMAX(ts, 0x142, 0xa);
    SMF_V(ap1, o1, wo[5]); DPPMAX(ts, 0x143, 0xc);
    SMF_V(ap2, o0, wo[2]); SMF_V(ap3, o0, wo[3]); SMF_V(ap2, o1, wo[6]);
    asm volatile("" : "+v"(ap0), "+v"(ap1));
    u32x4 pv;
    { float t0 = ap0[0] + ap0[2], t1 = ap1[0] + ap1[2]; KEEP(t0); KEEP(t1);
      SMF_V(ap3, o1, wo[7]);
      pv.x = max(pk2(t0, t1), 1u); pv.z = max(pk2(ap0[1] + ap0[3], ap1[1] + ap1[3]), 1u); }
    asm volatile("s_nop 4" : "+v"(pv.x), "+v"(pv.z), "+v"(ap2), "+v"(ap3));
    pv.y = max(pk2(ap2[0] + ap2[2], ap3[0] + ap3[2]), 1u); pv.w = max(pk2(ap2[1] + ap2[3], ap3[1] + ap3[3]), 1u);
    const unsigned sP = (s & 1) * 524288;
    st128(pv, rX, vPst, sP);
    ST(5);
    if (wave == 3) {
      const unsigned tp0 = clk32();
      const int tsm = __builtin_amdgcn_readlane(ts, 63);
      int dlt = tsm;
      asm volatile("s_max_i32 %0, %0, 0\n\ts_min_i32 %0, %0, 100\n\ts_mul_i32 %0, %0, 24" : "+s"(dlt) :: "scc");
      const bf16x8 asel = *(LAS bf16x8*)(lds + L_OI + lane * 16);
      FENCE();
      if (D1) __builtin_amdgcn_s_sleep(D1);
      wait_clk(tp0, TA + dlt);
      u32x4 r0 = ld128(rX, vPld, sP, 16), r1 = ld128(rX, vPld, sP + 1024, 16), r2 = ld128(rX, vPld, sP + 2048, 16), r3 = ld128(rX, vPld, sP + 3072, 16);
      if (__builtin_expect(__builtin_amdgcn_ballot_w64(min(min3u(min4u(r0), min4u(r1), min4u(r2)), min4u(r3)) == 0) != 0, 0)) {
        for (int spin = 0; spin < spin_limit; ++spin) {
          asm volatile("" ::: "memory");
          CNT(11);
          r0 = ld128(rX, vPld, sP, 16); r1 = ld128(rX, vPld, sP + 1024, 16); r2 = ld128(rX, vPld, sP + 2048, 16); r3 = ld128(rX, vPld, sP + 3072, 16);
          if (__builtin_amdgcn_ballot_w64(min(min3u(min4u(r0), min4u(r1), min4u(r2)), min4u(r3)) == 0) == 0) break;
          if (spin == spin_limit - 1) spin_limit = 0;
        }
      }
      ST(6);
      f32x4 d0;
      { const bf16x8 b0 = asb8(r0), b1 = asb8(r1), b2 = asb8(r2), b3 = asb8(r3);
        MFN0(d0, asel, b0); MF(d0, asel, b1); MF(d0, asel, b2); MF(d0, asel, b3); }
      PIN1(7, d0);
#pragma unroll
      for (int j = 0; j < 4; ++j) {
        float dj = d0[j];
        asm("v_add_f32_dpp %0, %0, %0 row_ror:8 row_mask:0xf bank_mask:0xf" : "+v"(dj));
        d0[j] = dj;
      }
      u32x2 mo = {max(pk2(d0[0], d0[1]), 1u), max(pk2(d0[2], d0[3]), 1u)};
      __builtin_amdgcn_raw_buffer_store_b64(mo, rX, vMst, ((s + 1) & 3) * 16384, 16);
      FENCE();
      const unsigned tp1 = clk32();
      ST(7);
      kv_commit();
      if (D2) __builtin_amdgcn_s_sleep(D2);
      wait_clk(tp1, TB);
    } else {
      kv_commit();
      if (wave == 0 && s + 2 < NSEG && kvknown < s + 3) {
        unsigned lo = vPz; asm volatile("" : "+v"(lo)); lo = (lo >> 4) & 3u;
        for (int spin = 0; spin < spin_limit; ++spin) {
          const int v = (int)__builtin_amdgcn_raw_buffer_load_b32(rX, (unsigned)(OFF_FLG + (F_KV + h) * 128) + lo * 4, 0, 16);
          kvknown = min(min(__builtin_amdgcn_readlane(v, 0), __builtin_amdgcn_readlane(v, 1)), min(__builtin_amdgcn_readlane(v, 2), __builtin_amdgcn_readlane(v, 3)));
          if (kvknown >= s + 3) break;
          asm volatile("" ::: "memory");
          if (spin == spin_limit - 1) spin_limit = 0;
        }
      }
    }
    lds_barrier();
    f0 = ld128(rX, vM, ((s + 1) & 3) * 16384, 16); f1 = ld128(rX, vM, ((s + 1) & 3) * 16384 + 4096, 16);
    FENCE();
    ST(8);
    aq0 = zero4(); aq1 = zero4(); ak0 = zero4(); ak1 = zero4(); av0 = zero4(); av1 = zero4();
#pragma unroll
    for (int n = 0; n < 8; ++n) kxf[n] = *(LAS bf16x8*)(lds + L_KX + n * 1024 + lane * 16);
    FENCE();
  }
  vmwait<0>();
  lds_barrier();
  __builtin_amdgcn_raw_buffer_store_b32((unsigned)NSEG, rX, vCH, 0, 16);
}

__global__ void __launch_bounds__(256, 1) k_main(Params p) {
  extern __shared__ __attribute__((aligned(16))) char smem[];
  LAS char* lds = (LAS char*)smem;
  const int b = blockIdx.x, x = b & 7, slot = b >> 3;
  int qi = -1;
  if (x < 4) {
    if (slot < 16) chain_role(p, lds, x * 8 + (slot >> 1), slot & 1);
    else if (slot < 18) qi = 8 + x * 2 + slot - 16;
  } else {
    const int y = x - 4;
    if (slot < 8) proj_role<0, 4>(p, lds, y * 8 + slot, 0, NSEG, F_KV + y * 8 + slot);
    else if (slot < 16) {
      const int i = y * 8 + slot - 8;
      if (i < 28) proj_role<2, 2>(p, lds, i % 7, (i / 7) * 16, (i / 7) * 16 + 16, F_Z1 + i);
      ah_role(p, lds, i);
    } else if (slot < 30) {
      sp_role(p, lds, y * 14 + slot - 16);
      lds_barrier();
      if (slot < 24) red_role(p, lds, y * 8 + slot - 16);
    } else qi = y * 2 + slot - 30;
  }
  if (qi >= 0) proj_role<1, 4>(p, lds, qi, 0, NSEG, F_Q + qi);
}

extern "C" void kernel_launch(void* const* d_in, const int* in_sizes, int n_in, void* d_out, int out_size, void* d_ws, size_t ws_size,
                              hipStream_t stream) {
  Params p;
  p.x = (const u16*)d_in[0]; p.mem0 = (const u16*)d_in[1]; p.Wq = (const u16*)d_in[2]; p.Wk = (const u16*)d_in[3];
  p.Wv = (const u16*)d_in[4]; p.Wo = (const u16*)d_in[5]; p.w_in = (const u16*)d_in[8]; p.w_sp = (const u16*)d_in[9];
  p.b_sp = (const u16*)d_in[10]; p.w_out = (const u16*)d_in[11]; p.out = (u16*)d_out;
  p.ws = (char*)(((uintptr_t)d_ws + (2u << 20) - 1) & ~(uintptr_t)((2u << 20) - 1));
  (void)hipFuncSetAttribute((const void*)k_main, hipFuncAttributeMaxDynamicSharedMemorySize, 163840);
  hipLaunchKernelGGL(k0_prep, dim3(258), dim3(256), 0, stream, p);
  hipLaunchKernelGGL(k_main, dim3(256), dim3(256), 163840, stream, p);
}
```

```cpp
#include <hip/hip_runtime.h>
#include <stdint.h>
#include <type_traits>

typedef __bf16 bf16x2 __attribute__((ext_vector_type(2)));
typedef __bf16 bf16x4 __attribute__((ext_vector_type(4)));
typedef __bf16 bf16x8 __attribute__((ext_vector_type(8)));
typedef __bf16 bf16x16 __attribute__((ext_vector_type(16)));
typedef float f32x4 __attribute__((ext_vector_type(4)));
typedef float f32x2 __attribute__((ext_vector_type(2)));
typedef unsigned u32x4 __attribute__((ext_vector_type(4)));
typedef unsigned u32x2 __attribute__((ext_vector_type(2)));
typedef unsigned short u16;
#define LAS __attribute__((address_space(3)))
#define GAS __attribute__((address_space(1)))
typedef __amdgpu_buffer_rsrc_t rsrc_t;

constexpr int S_ = 2048, DM = 512, NH = 32, DH = 128, NSEG = 64, FH = 896;
constexpr size_t OFF_X = 0;
constexpr size_t X_M = 1u << 20;
constexpr size_t X_TS = X_M + 65536;
constexpr size_t OFF_FLG = X_M + 131072;
constexpr size_t X_ZERO = OFF_FLG + 65536;
constexpr size_t OFF_XN = 4u << 20;
constexpr size_t OFF_KF = 8u << 20;
constexpr size_t OFF_VF = 24u << 20;
constexpr size_t OFF_QF = 40u << 20;
constexpr size_t OFF_KM = 56u << 20;
constexpr size_t OFF_VM = 60u << 20;
constexpr size_t OFF_Z1 = 64u << 20;
constexpr size_t OFF_Z2 = 68u << 20;
constexpr size_t OFF_ZN = 72u << 20;
constexpr size_t OFF_SL = 128u << 20;
constexpr int NSLAB = 39;
constexpr int FLG_SEL = 0;
constexpr int FLG_BYTES = 65536;
constexpr int F_KV = 2, F_Q = 34, F_CH = 72, F_AH = 136, F_LN = 168, F_SP = 232, F_Z2 = 288, F_Z1 = 344, F_XN = 376, F_SEL = 408;
constexpr int SPIN_LIMIT = 200000;

__device__ __forceinline__ rsrc_t mkrsrc(const void* p, unsigned bytes) { return __builtin_amdgcn_make_buffer_rsrc((void*)p, 0, bytes, 0x00020000); }
__device__ __forceinline__ u32x4 ld128(rsrc_t r, unsigned v, unsigned s, int aux) {
  return aux ? __builtin_amdgcn_raw_buffer_load_b128(r, v, s, 16) : __builtin_amdgcn_raw_buffer_load_b128(r, v, s, 0);
}
__device__ __forceinline__ void st128(u32x4 v, rsrc_t r, unsigned vo, unsigned so) {
  __builtin_amdgcn_raw_buffer_store_b128(v, r, vo, so, 16);
  asm volatile("s_nop 1" :: "v"(v));
}
__device__ __forceinline__ void reld2(rsrc_t r, unsigned v, unsigned s0, unsigned s1, u32x4& a, u32x4& b) {
  asm volatile("s_nop 4\n\tbuffer_load_dwordx4 %0, %2, %3, %4 offen sc1\n\tbuffer_load_dwordx4 %1, %2, %3, %5 offen sc1\n\ts_waitcnt vmcnt(0)"
               : "=&v"(a), "=&v"(b) : "v"(v), "s"(r), "s"(s0), "s"(s1) : "memory");
}
__device__ __forceinline__ void reld4(rsrc_t r, unsigned v, unsigned s0, u32x4& a, u32x4& b, u32x4& c, u32x4& d) {
  asm volatile("s_nop 4\n\tbuffer_load_dwordx4 %0, %4, %5, %6 offen sc1\n\tbuffer_load_dwordx4 %1, %4, %5, %6 offen offset:1024 sc1\n\t"
               "buffer_load_dwordx4 %2, %4, %5, %6 offen offset:2048 sc1\n\tbuffer_load_dwordx4 %3, %4, %5, %6 offen offset:3072 sc1\n\ts_waitcnt vmcnt(0)"
               : "=&v"(a), "=&v"(b), "=&v"(c), "=&v"(d) : "v"(v), "s"(r), "s"(s0) : "memory");
}
__device__ __forceinline__ unsigned pk2(float a, float b) {
  bf16x2 v = {(__bf16)a, (__bf16)b};
  return __builtin_bit_cast(unsigned, v);
}
__device__ __forceinline__ float bflo(unsigned u) { return __builtin_bit_cast(float, u << 16); }
__device__ __forceinline__ float bfhi(unsigned u) { return __builtin_bit_cast(float, u & 0xffff0000u); }
__device__ __forceinline__ void lds_barrier() { asm volatile("s_waitcnt lgkmcnt(0)\n\ts_barrier" ::: "memory"); }
__device__ __forceinline__ bf16x4 trrd(LAS char* p) { return __builtin_amdgcn_ds_read_tr16_b64_v4bf16((LAS bf16x4*)p); }
__device__ __forceinline__ bf16x8 cat8(bf16x4 a, bf16x4 b) { return __builtin_shufflevector(a, b, 0, 1, 2, 3, 4, 5, 6, 7); }
__device__ __forceinline__ bf16x16 cat16(bf16x8 a, bf16x8 b) { return __builtin_shufflevector(a, b, 0, 1, 2, 3, 4, 5, 6, 7, 8, 9, 10, 11, 12, 13, 14, 15); }
__device__ __forceinline__ bf16x8 asb8(u32x4 v) { return __builtin_bit_cast(bf16x8, v); }
__device__ __forceinline__ f32x4 mfma16(bf16x8 a, bf16x8 b, f32x4 c) { return __builtin_amdgcn_mfma_f32_16x16x32_bf16(a, b, c, 0, 0, 0); }
__device__ __forceinline__ f32x4 smfma(bf16x8 a, bf16x16 b, f32x4 c, int idx) { return __builtin_amdgcn_smfmac_f32_16x16x64_bf16(a, b, c, idx, 0, 0); }
__device__ __forceinline__ void glds16(const void* g, unsigned ldsoff) {
  unsigned keep;
  asm volatile("s_mov_b32 %0, m0\n\ts_mov_b32 m0, %2\n\ts_nop 0\n\tglobal_load_lds_dwordx4 %1, off\n\ts_mov_b32 m0, %0" : "=&s"(keep) : "v"(g), "s"(ldsoff) : "memory");
}
template <int IMM> __device__ __forceinline__ void glds_s(unsigned voff, const void* sbase, unsigned m0v) {
  asm volatile("s_mov_b32 m0, %2\n\ts_nop 0\n\tglobal_load_lds_dwordx4 %0, %1 offset:%3" :: "v"(voff), "s"(sbase), "s"(m0v), "i"(IMM));
}
template <int IMM> __device__ __forceinline__ void glds_c(unsigned voff, const void* sbase, unsigned m0v) {
  asm volatile("s_mov_b32 m0, %2\n\ts_nop 0\n\tglobal_load_lds_dwordx4 %0, %1 offset:%3 sc1" :: "v"(voff), "s"(sbase), "s"(m0v), "i"(IMM) : "memory");
}
template <int IMM> __device__ __forceinline__ void glds_d(unsigned voff, const void* sbase, unsigned m0v) {
  asm volatile("s_mov_b32 m0, %2\n\ts_nop 0\n\tglobal_load_lds_dwordx4 %0, %1 offset:%3 sc1" :: "v"(voff), "s"(sbase), "s"(m0v), "i"(IMM));
}
#define SB() __builtin_amdgcn_sched_barrier(0)
#define SMF_A(acc, a, b) asm volatile("v_smfmac_f32_16x16x64_bf16 %0, %1, %2, %3" : "+v"(acc) : "v"(a), "a"(b), "v"(sidx))
#define SMF_V(acc, a, b) asm volatile("v_smfmac_f32_16x16x64_bf16 %0, %1, %2, %3" : "+v"(acc) : "v"(a), "v"(b), "v"(sidx))
#define MF(acc, a, b) asm volatile("v_mfma_f32_16x16x32_bf16 %0, %1, %2, %0" : "+v"(acc) : "v"(a), "v"(b))
#define MF0(acc, a, b) asm volatile("v_mfma_f32_16x16x32_bf16 %0, %1, %2, 0" : "=&v"(acc) : "v"(a), "v"(b))
#define MFN(acc, a, b) asm volatile("s_nop 1\n\tv_mfma_f32_16x16x32_bf16 %0, %1, %2, %0" : "+v"(acc) : "v"(a), "v"(b))
#define MFN0(acc, a, b) asm volatile("s_nop 1\n\tv_mfma_f32_16x16x32_bf16 %0, %1, %2, 0" : "=&v"(acc) : "v"(a), "v"(b))
#define MFm(acc, a, b) asm volatile("v_mfma_f32_16x16x32_bf16 %0, %1, %2, %0" : "+v"(acc) : "v"(a), "v"(b) : "memory")
#define MF0m(acc, a, b) asm volatile("v_mfma_f32_16x16x32_bf16 %0, %1, %2, 0" : "=&v"(acc) : "v"(a), "v"(b) : "memory")
__device__ __forceinline__ f32x4 zero4() {
  f32x2 lo, hi;
  asm volatile("v_mov_b64 %0, 0\n\tv_mov_b64 %1, 0" : "=v"(lo), "=v"(hi));
  return __builtin_shufflevector(lo, hi, 0, 1, 2, 3);
}
#define PIN1(n, x) asm volatile("s_nop " #n : "+v"(x))
#define PIN2(n, x, y) asm volatile("s_nop " #n : "+v"(x), "+v"(y))
#define PIN4(n, x, y, z, w) asm volatile("s_nop " #n : "+v"(x), "+v"(y), "+v"(z), "+v"(w))
__device__ __forceinline__ unsigned clk32() { unsigned long long t; asm volatile("s_memtime %0\n\ts_waitcnt lgkmcnt(0)" : "=s"(t) :: "memory"); return (unsigned)t; }
__device__ __forceinline__ void wait_until(unsigned tgt) {
  asm volatile("s_memtime vcc\n\ts_waitcnt lgkmcnt(0)\n\ts_sub_i32 %0, %0, vcc_lo\n\ts_sub_i32 %0, %0, 60\n\ts_cmp_lt_i32 %0, 0\n\ts_cbranch_scc1 2f\n\t"
               "s_min_i32 %0, %0, 0x2000\n\ts_mul_i32 %0, %0, 585\n\ts_lshr_b32 %0, %0, 14\n\t.p2align 5\n1:\n\ts_sub_u32 %0, %0, 1\n\ts_cbranch_scc0 1b\n2:"
               : "+s"(tgt) :: "scc", "vcc", "memory");
}
__device__ __forceinline__ unsigned clock_offset() {
  unsigned long long r0, r1, r2, r3, r4, r5, r6, r7, r8, r9, m0;
  asm volatile("s_memtime %10\n\ts_memrealtime %0\n\ts_nop 1\n\ts_memrealtime %1\n\ts_nop 1\n\ts_memrealtime %2\n\ts_nop 1\n\ts_memrealtime %3\n\ts_nop 1\n\ts_memrealtime %4\n\ts_nop 1\n\t"
               "s_memrealtime %5\n\ts_nop 1\n\ts_memrealtime %6\n\ts_nop 1\n\ts_memrealtime %7\n\ts_nop 1\n\ts_memrealtime %8\n\ts_nop 1\n\ts_memrealtime %9\n\ts_waitcnt lgkmcnt(0)"
               : "=&s"(r0), "=&s"(r1), "=&s"(r2), "=&s"(r3), "=&s"(r4), "=&s"(r5), "=&s"(r6), "=&s"(r7), "=&s"(r8), "=&s"(r9), "=&s"(m0) :: "memory");
  const unsigned b = (unsigned)r0;
  const unsigned k = ((unsigned)r1 == b) + ((unsigned)r2 == b) + ((unsigned)r3 == b) + ((unsigned)r4 == b) + ((unsigned)r5 == b) + ((unsigned)r6 == b) +
                     ((unsigned)r7 == b) + ((unsigned)r8 == b) + ((unsigned)r9 == b);
  return (b + 4u) * 24u - ((unsigned)m0 + k * 12u);
}
__device__ __forceinline__ float max3f(float a, float b, float c) { float r; asm("v_max3_f32 %0, %1, %2, %3" : "=v"(r) : "v"(a), "v"(b), "v"(c)); return r; }
__device__ __forceinline__ float max2f(float a, float b) { float r; asm("v_max_f32 %0, %1, %2" : "=v"(r) : "v"(a), "v"(b)); return r; }
__device__ __forceinline__ void swap32(float x, float& r0, float& r1) {
  asm("v_mov_b32 %0, %2\n\tv_mov_b32 %1, %2\n\ts_nop 1\n\tv_permlane32_swap_b32 %0, %1" : "=&v"(r0), "=&v"(r1) : "v"(x));
}
__device__ __forceinline__ void swap16(float x, float& r0, float& r1) {
  asm("v_mov_b32 %0, %2\n\tv_mov_b32 %1, %2\n\ts_nop 1\n\tv_permlane16_swap_b32 %0, %1" : "=&v"(r0), "=&v"(r1) : "v"(x));
}
__device__ __forceinline__ float xmax_g(float x) {
  float t;
  asm("v_mov_b32 %1, %0\n\ts_nop 1\n\tv_permlane32_swap_b32 %0, %1\n\tv_max_f32 %0, %0, %1\n\tv_mov_b32 %1, %0\n\ts_nop 1\n\tv_permlane16_swap_b32 %0, %1\n\tv_max_f32 %0, %0, %1"
      : "+v"(x), "=&v"(t));
  return x;
}
__device__ __forceinline__ float xsum_g(float x) {
  float t;
  asm("v_mov_b32 %1, %0\n\ts_nop 1\n\tv_permlane32_swap_b32 %0, %1\n\tv_add_f32 %0, %0, %1\n\tv_mov_b32 %1, %0\n\ts_nop 1\n\tv_permlane16_swap_b32 %0, %1\n\tv_add_f32 %0, %0, %1"
      : "+v"(x), "=&v"(t));
  return x;
}

struct Params {
  const u16* x; const u16* mem0; const u16* Wq; const u16* Wk; const u16* Wv; const u16* Wo;
  const u16* w_in; const u16* w_sp; const u16* b_sp; const u16* w_out;
  u16* out; char* ws;
};

__device__ __forceinline__ float gelu_tanh(float x) {
  const float u = 0.7978845608028654f * (x + 0.044715f * x * x * x);
  const float e = __builtin_amdgcn_exp2f(u * 2.885390081777927f);
  const float th = 1.f - 2.f * __builtin_amdgcn_rcpf(e + 1.f);
  return 0.5f * x * (1.f + th);
}

#define MAPV 1
#define PSL 2
__device__ __forceinline__ void wait_flags(rsrc_t rF, unsigned off, unsigned want, int& budget) {
  const bool act = off != 0x80000000u;
  for (int spin = 0; spin < budget; ++spin) {
    const unsigned v = __builtin_amdgcn_raw_buffer_load_b32(rF, off, 0, 16);
    if (__builtin_amdgcn_ballot_w64(act && v < want) == 0) return;
    __builtin_amdgcn_s_sleep(PSL);
    asm volatile("" ::: "memory");
  }
  budget = 0;
}
__device__ __forceinline__ void put_flag(rsrc_t rF, unsigned off, unsigned v, int lane) {
  __builtin_amdgcn_raw_buffer_store_b32(v, rF, lane == 0 ? off : 0x80000000u, 0, 16);
}
template <int N> __device__ __forceinline__ void vmwait() { asm volatile("s_waitcnt vmcnt(%0)" :: "i"(N) : "memory"); }

template <int KIND, int NT>
__device__ __forceinline__ void proj_role(const Params& p, LAS char* lds, int j, int s0, int s1, int fline) {
  const int tid = threadIdx.x, lane = tid & 63, wave = __builtin_amdgcn_readfirstlane(tid >> 6);
  const int g = lane >> 4, c = lane & 15;
  char* ws = p.ws;
  const unsigned lbase = (unsigned)(uintptr_t)lds;
  constexpr int NG = 4 * NT, RB = 128 * NT, LPR = 8 * NT, RPI = 8 / NT;
  const char* xn = ws + OFF_XN;
  bf16x8 w[NT][16];
  const int ldw = (KIND >= 2) ? 2 * FH : NH * DH;
  auto rotf = [](int row) { return NT == 1 ? ((row >> 1) & 1) + 2 * ((row >> 3) & 1) : (row & 3) + 4 * ((row >> 3) & 1); };
  constexpr int NBUF = NT == 4 ? 2 : 4, SI = 4 * NT;
  auto wissue = [&](int st) {
#pragma unroll 4
    for (int n = 0; n < SI; ++n) {
      const int row = (wave * SI + n) * RPI + lane / LPR;
      const int x = lane % LPR;
      const int G = ((x >> 1) - rotf(row)) & (NG - 1);
      const int k = st * 128 + row;
      const u16* src;
      if (KIND == 0) src = (G < 8 ? p.Wk : p.Wv) + (size_t)k * ldw + j * 128 + (G & 7) * 16 + (x & 1) * 8;
      else if (KIND == 1) src = p.Wq + (size_t)k * ldw + j * 256 + G * 16 + (x & 1) * 8;
      else if (KIND == 2) src = p.w_in + (size_t)k * ldw + j * 128 + G * 16 + (x & 1) * 8;
      else src = p.w_in + (size_t)k * ldw + FH + j * 64 + G * 16 + (x & 1) * 8;
      glds16(src, lbase + (st % NBUF) * 128 * RB + (wave * SI + n) * 1024);
    }
  };
#pragma unroll
  for (int st = 0; st < NBUF; ++st) wissue(st);
#pragma unroll
  for (int st = 0; st < 4; ++st) {
    if (NBUF == 2) { if (st < 3) vmwait<SI>(); else vmwait<0>(); }
    else { if (st == 0) vmwait<3 * SI>(); else if (st == 1) vmwait<2 * SI>(); else if (st == 2) vmwait<SI>(); else vmwait<0>(); }
    lds_barrier();
#pragma unroll
    for (int ks = 0; ks < 4; ++ks)
#pragma unroll
      for (int t = 0; t < NT; ++t) {
        bf16x4 pc[2];
#pragma unroll
        for (int u = 0; u < 2; ++u) {
          const int row = 32 * ks + 8 * g + 4 * u + (c >> 2);
          pc[u] = trrd(lds + (st % NBUF) * 128 * RB + row * RB + ((wave * NT + t + rotf(row)) & (NG - 1)) * 32 + (c & 3) * 8);
        }
        w[t][st * 4 + ks] = cat8(pc[0], pc[1]);
      }
    if (NBUF == 2 && st < 2) { lds_barrier(); wissue(st + 2); }
  }
  lds_barrier();
#pragma unroll
  for (int t = 0; t < NT; ++t)
#pragma unroll
    for (int k = 0; k < 16; ++k) asm volatile("" : "+a"(w[t][k]));
  const unsigned l16 = lane * 16;
  auto piece = [&](int s, int n) {
    const char* sb = xn + (size_t)s * 32768 + wave * 8192 + (n >> 2) * 4096;
    const unsigned m0v = lbase + (s % 3) * 32768 + wave * 8192 + (n >> 2) * 4096;
    if ((n & 3) == 0) glds_d<0>(l16, sb, m0v); else if ((n & 3) == 1) glds_d<1024>(l16, sb, m0v); else if ((n & 3) == 2) glds_d<2048>(l16, sb, m0v); else glds_d<3072>(l16, sb, m0v);
  };
  const rsrc_t rF = mkrsrc(ws + OFF_FLG, FLG_BYTES);
  int budget = SPIN_LIMIT;
  wait_flags(rF, (lane >= s0 && lane < s0 + 3) ? (unsigned)(F_XN * 128 + lane * 4) : 0x80000000u, 1, budget);
#pragma unroll
  for (int n = 0; n < 8; ++n) piece(s0, n);
#pragma unroll
  for (int n = 0; n < 8; ++n) piece(s0 + 1, n);
  const unsigned foff = fline * 128 + wave * 4;
  const rsrc_t rO = KIND == 0 ? mkrsrc(ws + (wave >= 2 ? OFF_VF : OFF_KF) + (size_t)j * NSEG * 8192 + (wave & 1) * 4096, NSEG * 8192)
                  : KIND == 1 ? mkrsrc(ws + OFF_QF + (size_t)(2 * j + (wave >> 1)) * NSEG * 8192 + (wave & 1) * 4096, NSEG * 8192)
                  : KIND == 2 ? mkrsrc(ws + OFF_Z1 + (size_t)(j * 4 + wave) * 1024, NSEG * 57344) : mkrsrc(ws + OFF_Z2 + (size_t)(j * 4 + wave) * 1024, NSEG * 57344);
  auto run = [&](auto vf) {
    constexpr bool vform = decltype(vf)::value;
    auto emit = [&](f32x4 (&prv)[2][NT], int s, int ch) {
      if (KIND >= 2) {
#pragma unroll
        for (int q = 0; q < NT; ++q) { const int e = ch * NT + q; prv[e / (4 * NT)][(e / 4) % NT][e & 3] = gelu_tanh(prv[e / (4 * NT)][(e / 4) % NT][e & 3]); }
        if (KIND == 2 && (ch == 3 || ch == 7)) {
          const int T = ch >> 2; const f32x4 a0 = prv[T][0], a1 = prv[T][NT - 1];
          st128((u32x4){pk2(a0[0], a1[0]), pk2(a0[1], a1[1]), pk2(a0[2], a1[2]), pk2(a0[3], a1[3])}, rO, l16, s * 57344 + T * 28672);
        }
        if (KIND == 3 && ch == 7)
          st128((u32x4){pk2(prv[0][0][0], prv[0][0][1]), pk2(prv[0][0][2], prv[0][0][3]), pk2(prv[1][0][0], prv[1][0][1]), pk2(prv[1][0][2], prv[1][0][3])}, rO, l16, s * 57344);
      } else if (ch < 4) {
        const int t = ch % NT;
        u32x4 o;
        if (vform) o = (u32x4){pk2(prv[0][t][0], prv[0][t][1]), pk2(prv[0][t][2], prv[0][t][3]), pk2(prv[1][t][0], prv[1][t][1]), pk2(prv[1][t][2], prv[1][t][3])};
        else { const f32x4 a0 = prv[ch & 1][(ch & 2) % NT], a1 = prv[ch & 1][((ch & 2) + 1) % NT];
               o = (u32x4){pk2(a0[0], a1[0]), pk2(a0[1], a1[1]), pk2(a0[2], a1[2]), pk2(a0[3], a1[3])}; }
        st128(o, rO, l16, s * 8192 + ch * 1024);
      }
    };
    auto mm = [&](f32x4& acc, const bf16x8& wv, const bf16x8& xv, bool first) {
      if (vform) { if (first) asm volatile("v_mfma_f32_16x16x32_bf16 %0, %1, %2, 0" : "=&v"(acc) : "v"(xv), "a"(wv) : "memory");
                   else asm volatile("v_mfma_f32_16x16x32_bf16 %0, %1, %2, %0" : "+v"(acc) : "v"(xv), "a"(wv) : "memory"); }
      else { if (first) asm volatile("v_mfma_f32_16x16x32_bf16 %0, %1, %2, 0" : "=&v"(acc) : "a"(wv), "v"(xv) : "memory");
             else asm volatile("v_mfma_f32_16x16x32_bf16 %0, %1, %2, %0" : "+v"(acc) : "a"(wv), "v"(xv) : "memory"); }
    };
    auto body = [&](auto em, auto pc, f32x4 (&acc)[2][NT], f32x4 (&prv)[2][NT], int s, int slot) {
      constexpr bool EM = decltype(em)::value, PC = decltype(pc)::value;
      LAS char* src = lds + slot * 32768 + lane * 16;
      const char* sb = xn + (size_t)(s + 2) * 32768 + wave * 8192;
      const unsigned m0v = lbase + (slot == 0 ? 2 : slot - 1) * 32768 + wave * 8192;
      bf16x8 xf[2][4];
#pragma unroll
      for (int q = 0; q < 4; ++q) xf[0][q] = *(LAS bf16x8*)(src + ((q & 1) * 16 + (q >> 1)) * 1024);
      if (EM) {
#pragma unroll
        for (int t = 0; t < NT; ++t) asm volatile("s_nop 3" : "+v"(prv[0][t]), "+v"(prv[1][t]) :: "memory");
      }
#pragma unroll
      for (int ch = 0; ch < 8; ++ch) {
#pragma unroll
        for (int i = 0; i < 4 * NT; ++i) {
          const int q = i / NT, t = i % NT;
          mm(acc[q & 1][t], w[t][2 * ch + (q >> 1)], xf[ch & 1][q], ch == 0 && q < 2);
          if (i < 4 && ch < 7) xf[(ch + 1) & 1][i] = *(LAS bf16x8*)(src + ((i & 1) * 16 + 2 * (ch + 1) + (i >> 1)) * 1024);
          if (EM && i == (NT == 4 ? 5 : NT == 2 ? 4 : 1)) emit(prv, s - 1, ch);
          if (PC && i == (NT == 4 ? 10 : NT == 2 ? 6 : 2)) {
            if (ch == 0) glds_d<0>(l16, sb, m0v); else if (ch == 1) glds_d<1024>(l16, sb, m0v); else if (ch == 2) glds_d<2048>(l16, sb, m0v); else if (ch == 3) glds_d<3072>(l16, sb, m0v);
            else if (ch == 4) glds_d<0>(l16, sb + 4096, m0v + 4096); else if (ch == 5) glds_d<1024>(l16, sb + 4096, m0v + 4096);
            else if (ch == 6) glds_d<2048>(l16, sb + 4096, m0v + 4096); else glds_d<3072>(l16, sb + 4096, m0v + 4096);
          }
        }
      }
    };
    auto settle = [&](f32x4 (&acc)[2][NT]) {
#pragma unroll
      for (int t = 0; t < NT; ++t) asm volatile("s_nop 7" : "+v"(acc[0][t]), "+v"(acc[1][t]));
    };
    const std::true_type Y{}; const std::false_type N{};
    f32x4 accA[2][NT], accB[2][NT];
    int slot = s0 % 3;
    auto nxt = [&]() { slot = slot == 2 ? 0 : slot + 1; };
    vmwait<8>(); lds_barrier();
    body(N, Y, accA, accB, s0, slot); nxt(); settle(accA);
#pragma unroll
    for (int ch = 0; ch < 8; ++ch) emit(accA, s0, ch);
    vmwait<0>(); put_flag(rF, foff, s0 + 1, lane);
    wait_flags(rF, (lane >= s0 && lane < s1) ? (unsigned)(F_XN * 128 + lane * 4) : 0x80000000u, 1, budget);
    lds_barrier();
    body(N, Y, accB, accA, s0 + 1, slot); nxt(); settle(accB);
#pragma unroll
    for (int ch = 0; ch < 8; ++ch) emit(accB, s0 + 1, ch);
    vmwait<0>(); put_flag(rF, foff, s0 + 2, lane);
    lds_barrier();
    body(N, Y, accA, accB, s0 + 2, slot); nxt();
    vmwait<8>(); lds_barrier();
    put_flag(rF, foff, s0 + 2, lane);
    body(Y, Y, accB, accA, s0 + 3, slot); nxt();
    int s = s0 + 4;
    for (; s + 3 < s1; s += 2) {
      if (s == s0 + 4) vmwait<0>(); else vmwait<9 + NT>();
      lds_barrier();
      put_flag(rF, foff, s == s0 + 4 ? s - 1 : s - 2, lane);
      body(Y, Y, accA, accB, s, slot); nxt();
      vmwait<9 + NT>(); lds_barrier();
      put_flag(rF, foff, s - 1, lane);
      body(Y, Y, accB, accA, s + 1, slot); nxt();
    }
    vmwait<9 + NT>(); lds_barrier();
    put_flag(rF, foff, s - 2, lane);
    body(Y, N, accA, accB, s, slot); nxt();
    vmwait<0>(); lds_barrier();
    put_flag(rF, foff, s, lane);
    body(Y, N, accB, accA, s + 1, slot); settle(accB);
#pragma unroll
    for (int ch = 0; ch < 8; ++ch) emit(accB, s + 1, ch);
  };
  if ((KIND == 0 && wave >= 2) || KIND == 3) run(std::true_type{}); else run(std::false_type{});
  vmwait<0>();
  put_flag(rF, foff, s1, lane);
  lds_barrier();
}

__device__ __forceinline__ float xmax_g(float x);
__device__ __forceinline__ float xsum_g(float x);
__device__ __forceinline__ void ah_role(const Params& p, LAS char* lds, int h) {
  const int tid = threadIdx.x, lane = tid & 63, wave = __builtin_amdgcn_readfirstlane(tid >> 6);
  const int g = lane >> 4, c = lane & 15;
  char* ws = p.ws;
  const unsigned lbase = (unsigned)(uintptr_t)lds;
#pragma unroll 4
  for (int n = 0; n < 32; ++n) {
    const int e = wave * 32 + n;
    const int G = ((lane >> 2) - (e & 3)) & 15;
    glds16(p.Wo + (size_t)(h * DH + e) * DM + G * 32 + (lane & 3) * 8, lbase + e * 1024);
  }
  vmwait<0>();
  lds_barrier();
  bf16x8 wo[4][2][4];
#pragma unroll
  for (int P = 0; P < 4; ++P)
#pragma unroll
    for (int t = 0; t < 2; ++t)
#pragma unroll
      for (int m = 0; m < 4; ++m) {
        bf16x4 pc[2];
#pragma unroll
        for (int u = 0; u < 2; ++u) {
          const int e = 32 * m + 16 * u + 4 * g + (c >> 2);
          pc[u] = trrd(lds + e * 1024 + ((wave * 4 + P + (e & 3)) & 15) * 64 + (c & 3) * 16 + t * 8);
        }
        wo[P][t][m] = cat8(pc[0], pc[1]);
      }
  lds_barrier();
  const rsrc_t rF = mkrsrc(ws + OFF_FLG, FLG_BYTES);
  const rsrc_t rK = mkrsrc(ws + OFF_KF + (size_t)h * NSEG * 8192, NSEG * 8192);
  const rsrc_t rV = mkrsrc(ws + OFF_VF + (size_t)h * NSEG * 8192, NSEG * 8192);
  const rsrc_t rQ = mkrsrc(ws + OFF_QF + (size_t)h * NSEG * 8192, NSEG * 8192);
  const rsrc_t rKM = mkrsrc(ws + OFF_KM + (size_t)h * NSEG * 2048, NSEG * 2048);
  const rsrc_t rVM = mkrsrc(ws + OFF_VM + (size_t)h * NSEG * 2048, NSEG * 2048);
  const rsrc_t rS = mkrsrc(ws + OFF_SL, NSEG * NSLAB * 32768);
  const int T = wave & 1, eh = wave >> 1;
  const unsigned fAll = lane < 4 ? (unsigned)((F_KV + h) * 128 + lane * 4) : lane < 6 ? (unsigned)((F_Q + (h >> 1)) * 128 + ((h & 1) * 2 + lane - 4) * 4)
                      : lane < 8 ? (unsigned)((F_CH + 2 * h + lane - 6) * 128) : 0x80000000u;
  const int tq = 8 * (c >> 2) + 4 * T + (c & 3);
  const float CEXP = 0.08838834764831845f * 1.4426950408889634f;
  int budget = SPIN_LIMIT;
  u32x4 pf[6];
  auto issue = [&](int s) {
    const unsigned so = s * 8192 + wave * 2048;
    pf[0] = ld128(rK, lane * 16, so, 16); pf[1] = ld128(rK, lane * 16, so + 1024, 16);
    pf[2] = ld128(rQ, lane * 16, so, 16); pf[3] = ld128(rQ, lane * 16, so + 1024, 16);
    pf[4] = ld128(rV, lane * 16, so, 16); pf[5] = ld128(rV, lane * 16, so + 1024, 16);
  };
  auto commit = [&](int s) {
    LAS char* d = lds + 16384 + (s & 1) * 24576 + wave * 2048 + lane * 16;
#pragma unroll
    for (int n = 0; n < 6; ++n) *(LAS u32x4*)(d + (n >> 1) * 8192 + (n & 1) * 1024) = pf[n];
  };
  u32x4 km[4]; unsigned vm[4];
  auto memfetch = [&](int s) {
#pragma unroll
    for (int k4 = 0; k4 < 4; ++k4) km[k4] = ld128(rKM, (c & 1) * 256 + g * 64 + (c >> 2) * 16, s * 2048 + k4 * 512, 16);
#pragma unroll
    for (int n = 0; n < 4; ++n) vm[n] = __builtin_amdgcn_raw_buffer_load_b32(rVM, lane * 4, s * 2048 + (eh * 4 + n) * 256, 16);
  };
  wait_flags(rF, lane < 6 ? fAll : 0x80000000u, 2, budget);
  issue(0); commit(0); issue(1);
  lds_barrier();
  bool have = false;
  unsigned fl = 0;
#define AS(i)
  for (int s = 0; s < NSEG; ++s) {
    LAS char* in = lds + 16384 + (s & 1) * 24576 + lane * 16;
    bf16x8 qx[4], kx[8], vx[4];
#pragma unroll
    for (int k4 = 0; k4 < 4; ++k4) {
      qx[k4] = *(LAS bf16x8*)(in + 8192 + (k4 * 2 + T) * 1024);
      kx[2 * k4] = *(LAS bf16x8*)(in + (k4 * 2) * 1024); kx[2 * k4 + 1] = *(LAS bf16x8*)(in + (k4 * 2 + 1) * 1024);
    }
#pragma unroll
    for (int n = 0; n < 4; ++n) vx[n] = *(LAS bf16x8*)(in + 16384 + (eh * 4 + n) * 1024);
    SB();
    f32x4 sx[2] = {}, sm = {};
#pragma unroll
    for (int k4 = 0; k4 < 4; ++k4) { sx[0] = mfma16(kx[2 * k4], qx[k4], sx[0]); sx[1] = mfma16(kx[2 * k4 + 1], qx[k4], sx[1]); }
    if (!have) {
      vmwait<0>();
      if (s) put_flag(rF, (F_AH + h) * 128 + wave * 4, s, lane);
      for (int spin = 0; spin < budget; ++spin) {
        const unsigned f2 = __builtin_amdgcn_raw_buffer_load_b32(rF, fAll, 0, 16);
        if (__builtin_amdgcn_ballot_w64(lane >= 6 && lane < 8 && f2 < (unsigned)(s + 1)) == 0) break;
        __builtin_amdgcn_s_sleep(PSL);
        asm volatile("" ::: "memory");
        if (spin == budget - 1) budget = 0;
      }
      memfetch(s);
      asm volatile("" : "+v"(km[0]), "+v"(km[1]), "+v"(km[2]), "+v"(km[3]), "+v"(vm[0]), "+v"(vm[1]), "+v"(vm[2]), "+v"(vm[3]));
    }
    AS(0);
    float v[10];
#pragma unroll
    for (int T2 = 0; T2 < 2; ++T2)
#pragma unroll
      for (int r = 0; r < 4; ++r) v[T2 * 4 + r] = (8 * g + 4 * T2 + r <= tq) ? sx[T2][r] : -3.0e38f;
#pragma unroll
    for (int k4 = 0; k4 < 4; ++k4) sm = mfma16(asb8(km[k4]), qx[k4], sm);
    v[8] = sm[0]; v[9] = sm[1];
    float mm = v[0];
#pragma unroll
    for (int r = 1; r < 10; ++r) mm = fmaxf(mm, v[r]);
    mm = xmax_g(mm);
    float den = 0.f;
#pragma unroll
    for (int r = 0; r < 10; ++r) { v[r] = __builtin_amdgcn_exp2f((v[r] - mm) * CEXP); den += v[r]; }
    const float rinv = __builtin_amdgcn_rcpf(xsum_g(den));
    const bf16x8 pb = asb8((u32x4){pk2(v[0] * rinv, v[1] * rinv), pk2(v[2] * rinv, v[3] * rinv), pk2(v[4] * rinv, v[5] * rinv), pk2(v[6] * rinv, v[7] * rinv)});
    const bf16x8 pm = asb8((u32x4){pk2(v[8] * rinv, v[9] * rinv), 0, 0, 0});
    f32x4 o[4];
#pragma unroll
    for (int n = 0; n < 4; ++n) {
      o[n] = mfma16(vx[n], pb, (f32x4){});
      o[n] = mfma16(asb8((u32x4){vm[n], 0, 0, 0}), pm, o[n]);
    }
    LAS char* ob = lds + (s & 1) * 8192;
#pragma unroll
    for (int m2 = 0; m2 < 2; ++m2)
      *(LAS u32x4*)(ob + (T * 4 + eh * 2 + m2) * 1024 + lane * 16) =
          (u32x4){pk2(o[2 * m2][0], o[2 * m2][1]), pk2(o[2 * m2][2], o[2 * m2][3]), pk2(o[2 * m2 + 1][0], o[2 * m2 + 1][1]), pk2(o[2 * m2 + 1][2], o[2 * m2 + 1][3])};
    AS(1);
    const bool pub = have;
    asm volatile("s_waitcnt vmcnt(0)" : "+v"(fl) :: "memory");
    have = false;
    AS(2);
    if (s + 1 < NSEG) commit(s + 1);
    asm volatile("" ::: "memory");
    if (pub) put_flag(rF, (F_AH + h) * 128 + wave * 4, s, lane);
    if (s + 1 < NSEG) {
      AS(6);
      have = __builtin_amdgcn_ballot_w64(lane >= 6 && lane < 8 && fl < (unsigned)(s + 2)) == 0;
      if (have) memfetch(s + 1);
      if (s + 2 < NSEG) {
        if (__builtin_amdgcn_ballot_w64(lane < 6 && fl < (unsigned)(s + 3)) != 0) wait_flags(rF, lane < 6 ? fAll : 0x80000000u, s + 3, budget);
        issue(s + 2);
      }
      fl = __builtin_amdgcn_raw_buffer_load_b32(rF, fAll, 0, 16);
    }
    AS(7);
    lds_barrier();
    AS(3);
    bf16x8 of[8];
#pragma unroll
    for (int n = 0; n < 8; ++n) of[n] = *(LAS bf16x8*)(ob + n * 1024 + lane * 16);
    f32x4 a0[2], a1[2];
#pragma unroll
    for (int i = 0; i <= 8; ++i) {
      if (i < 8) {
        const int T2 = i >> 2, P = i & 3;
        MF0(a0[i & 1], wo[P][0][0], of[T2 * 4]); MF0(a1[i & 1], wo[P][1][0], of[T2 * 4]);
        MF(a0[i & 1], wo[P][0][1], of[T2 * 4 + 1]); MF(a1[i & 1], wo[P][1][1], of[T2 * 4 + 1]);
      }
      if (i > 0) {
        const int j = i - 1;
        if (i == 8) asm volatile("s_nop 7" : "+v"(a0[j & 1]), "+v"(a1[j & 1])); else asm volatile("" : "+v"(a0[j & 1]), "+v"(a1[j & 1]));
        const f32x4 b0 = a0[j & 1], b1 = a1[j & 1];
        st128((u32x4){pk2(b0[0], b0[1]), pk2(b0[2], b0[3]), pk2(b1[0], b1[1]), pk2(b1[2], b1[3])}, rS, lane * 16, (s * NSLAB + h) * 32768 + (((j >> 2) * 4 + wave) * 4 + (j & 3)) * 1024);
      }
      if (i < 8) {
        const int T2 = i >> 2, P = i & 3;
        MF(a0[i & 1], wo[P][0][2], of[T2 * 4 + 2]); MF(a1[i & 1], wo[P][1][2], of[T2 * 4 + 2]);
        MF(a0[i & 1], wo[P][0][3], of[T2 * 4 + 3]); MF(a1[i & 1], wo[P][1][3], of[T2 * 4 + 3]);
      }
    }
    AS(4);
  }
  vmwait<0>();
  put_flag(rF, (F_AH + h) * 128 + wave * 4, NSEG, lane);
}

__device__ __forceinline__ void red_role(const Params& p, LAS char* lds, int r) {
  const int tid = threadIdx.x, lane = tid & 63, wave = __builtin_amdgcn_readfirstlane(tid >> 6);
  const int g = lane >> 4, c = lane & 15;
  char* ws = p.ws;
  const rsrc_t rF = mkrsrc(ws + OFF_FLG, FLG_BYTES);
  const rsrc_t rS = mkrsrc(ws + OFF_SL, NSEG * NSLAB * 32768);
  const int T = r >> 4, w = (r >> 2) & 3, P = r & 3;
  bf16x8 a0, a1;
#pragma unroll
  for (int j = 0; j < 8; ++j) { a0[j] = (__bf16)((8 * g + j == c) ? 1.f : 0.f); a1[j] = (__bf16)((8 * g + j == 16 + c) ? 1.f : 0.f); }
  const unsigned fA = lane < 8 ? (unsigned)((F_AH + wave * 8 + lane) * 128 + w * 4) : 0x80000000u;
  const int tok = 8 * (c >> 2) + 4 * T + (c & 3);
  int budget = SPIN_LIMIT;
  unsigned fl = 0;
  u32x4 v[10]; u32x2 xv = {0, 0};
  auto fetch = [&](int s) {
    if ((s & 7) == 0) {
      const unsigned fY = (lane < 8 && wave + 4 * (lane >> 2) < 7) ? (unsigned)((F_SP + (s >> 3) * 7 + wave + 4 * (lane >> 2)) * 128 + (lane & 3) * 4) : 0x80000000u;
      wait_flags(rF, fY, 1, budget);
    }
    const unsigned sb = s * NSLAB * 32768 + r * 1024;
    v[8] = ld128(rS, lane * 16, sb + (32 + wave) * 32768, 16);
    v[9] = ld128(rS, wave < 3 ? (unsigned)(lane * 16) : 0x80000000u, sb + (36 + (wave < 3 ? wave : 0)) * 32768, 16);
    if (wave < 2) xv = *(const u32x2*)(p.x + (size_t)(s * 32 + tok) * DM + 128 * w + 32 * P + 16 * wave + 4 * g);
    for (int spin = 0; __builtin_amdgcn_ballot_w64(lane < 8 && fl < (unsigned)(s + 1)) != 0 && spin < budget; ++spin) {
      if (spin) __builtin_amdgcn_s_sleep(PSL);
      asm volatile("" ::: "memory");
      fl = __builtin_amdgcn_raw_buffer_load_b32(rF, fA, 0, 16);
      if (spin == budget - 1) budget = 0;
    }
#pragma unroll
    for (int n = 0; n < 8; ++n) v[n] = ld128(rS, lane * 16, sb + (wave * 8 + n) * 32768, 16);
  };
  fetch(0);
  for (int s = 0; s < NSEG; ++s) {
    f32x4 d0 = {}, d1 = {};
#pragma unroll
    for (int n = 9; n >= 0; --n) { const int q = n >= 8 ? n : 7 - n; d0 = mfma16(a0, asb8(v[q]), d0); d1 = mfma16(a1, asb8(v[q]), d1); }
    const u32x2 xc = xv;
    LAS char* b = lds + (s & 1) * 8192;
    *(LAS f32x4*)(b + (wave * 2) * 1024 + lane * 16) = d0;
    *(LAS f32x4*)(b + (wave * 2 + 1) * 1024 + lane * 16) = d1;
    const bool ahead = ((s + 1) & 7) != 0 && __builtin_amdgcn_ballot_w64(lane < 8 && fl < (unsigned)(s + 2)) == 0;
    if (ahead) fetch(s + 1);
    lds_barrier();
    if (wave < 2) {
      f32x4 t = *(LAS f32x4*)(b + wave * 1024 + lane * 16);
#pragma unroll
      for (int n = 1; n < 4; ++n) t += *(LAS f32x4*)(b + (n * 2 + wave) * 1024 + lane * 16);
      *(u32x2*)(p.out + (size_t)(s * 32 + tok) * DM + 128 * w + 32 * P + 16 * wave + 4 * g) =
          (u32x2){pk2(t[0] + bflo(xc.x), t[1] + bfhi(xc.x)), pk2(t[2] + bflo(xc.y), t[3] + bfhi(xc.y))};
    }
    if (!ahead && s + 1 < NSEG) fetch(s + 1);
  }
}

__device__ __forceinline__ float rowsum16(float x) {
  x += __builtin_bit_cast(float, __builtin_amdgcn_update_dpp(0, __builtin_bit_cast(int, x), 0x128, 0xf, 0xf, false));
  x += __builtin_bit_cast(float, __builtin_amdgcn_update_dpp(0, __builtin_bit_cast(int, x), 0x124, 0xf, 0xf, false));
  x += __builtin_bit_cast(float, __builtin_amdgcn_update_dpp(0, __builtin_bit_cast(int, x), 0x122, 0xf, 0xf, false));
  x += __builtin_bit_cast(float, __builtin_amdgcn_update_dpp(0, __builtin_bit_cast(int, x), 0x121, 0xf, 0xf, false));
  return x;
}
__device__ __forceinline__ void sp_role(const Params& p, LAS char* lds, int tile) {
  const int tid = threadIdx.x, lane = tid & 63, wave = __builtin_amdgcn_readfirstlane(tid >> 6);
  const int g = lane >> 4, c = lane & 15;
  char* ws = p.ws;
  const unsigned lbase = (unsigned)(uintptr_t)lds;
  const rsrc_t rF = mkrsrc(ws + OFF_FLG, FLG_BYTES);
  int budget = SPIN_LIMIT;
  proj_role<3, 1>(p, lds, tile % 14, (tile / 14) * 16, (tile / 14) * 16 + 16, F_Z2 + tile);
  for (int s = tile; s < NSEG; s += 56) {
    const rsrc_t rZ = mkrsrc(ws + OFF_Z2 + (size_t)s * 57344, 57344);
    const rsrc_t rN = mkrsrc(ws + OFF_ZN + (size_t)s * 57344, 57344);
    wait_flags(rF, lane < 14 ? (unsigned)((F_Z2 + (s >> 4) * 14 + ((wave * 14 + lane) >> 2)) * 128 + ((wave * 14 + lane) & 3) * 4) : 0x80000000u, s + 1, budget);
    u32x4 z[14];
#pragma unroll
    for (int n = 0; n < 14; ++n) z[n] = ld128(rZ, lane * 16, (wave * 14 + n) * 1024, 16);
    float f[14][8], sum[8] = {};
#pragma unroll
    for (int n = 0; n < 14; ++n) {
      f[n][0] = bflo(z[n].x); f[n][1] = bfhi(z[n].x); f[n][2] = bflo(z[n].y); f[n][3] = bfhi(z[n].y);
      f[n][4] = bflo(z[n].z); f[n][5] = bfhi(z[n].z); f[n][6] = bflo(z[n].w); f[n][7] = bfhi(z[n].w);
#pragma unroll
      for (int j = 0; j < 8; ++j) sum[j] += f[n][j];
    }
    LAS float* red = (LAS float*)lds;
#pragma unroll
    for (int j = 0; j < 8; ++j) { const float t = rowsum16(sum[j]); if (c == 0) red[wave * 32 + g * 8 + j] = t; }
    lds_barrier();
    float q[8] = {};
#pragma unroll
    for (int j = 0; j < 8; ++j) {
      const float mean = (red[g * 8 + j] + red[32 + g * 8 + j] + red[64 + g * 8 + j] + red[96 + g * 8 + j]) * (1.f / FH);
#pragma unroll
      for (int n = 0; n < 14; ++n) { f[n][j] -= mean; q[j] += f[n][j] * f[n][j]; }
    }
#pragma unroll
    for (int j = 0; j < 8; ++j) { const float t = rowsum16(q[j]); if (c == 0) red[128 + wave * 32 + g * 8 + j] = t; }
    lds_barrier();
    float rs[8];
#pragma unroll
    for (int j = 0; j < 8; ++j)
      rs[j] = 1.f / sqrtf((red[128 + g * 8 + j] + red[160 + g * 8 + j] + red[192 + g * 8 + j] + red[224 + g * 8 + j]) * (1.f / FH) + 1e-5f);
#pragma unroll
    for (int n = 0; n < 14; ++n)
      st128((u32x4){pk2(f[n][0] * rs[0], f[n][1] * rs[1]), pk2(f[n][2] * rs[2], f[n][3] * rs[3]), pk2(f[n][4] * rs[4], f[n][5] * rs[5]), pk2(f[n][6] * rs[6], f[n][7] * rs[7])},
            rN, lane * 16, (wave * 14 + n) * 1024);
    vmwait<0>();
    lds_barrier();
    put_flag(rF, (F_LN + s) * 128, 1, tid);
  }
  const int tp = tile / 7, fc = tile % 7;
  if (wave == 0) {
    wait_flags(rF, (unsigned)((F_LN + lane) * 128), 1, budget);
    __builtin_amdgcn_fence(__ATOMIC_ACQUIRE, "agent");
    vmwait<0>();
  }
  lds_barrier();
  const char* zn = ws + OFF_ZN;
  unsigned boff[4];
#pragma unroll
  for (int Tt = 0; Tt < 4; ++Tt) boff[Tt] = (unsigned)(((Tt >> 1) * 32 + 8 * (c >> 2) + 4 * (Tt & 1) + (c & 3)) * S_ + 8 * g) * 2;
  const char* wsp = (const char*)(p.w_sp + (size_t)(tp * 256 + wave * 64) * S_);
  const unsigned l16 = lane * 16;
  auto piece = [&](int kk, int n) {
    const unsigned dst = lbase + (kk % 3) * 49152;
    if (n < 4) {
      const char* sb = zn + ((size_t)(kk * 2 + (wave >> 1)) * 56 + fc * 8 + (wave & 1) * 4) * 1024;
      const unsigned m0v = dst + wave * 4096;
      if (n == 0) glds_s<0>(l16, sb, m0v); else if (n == 1) glds_s<1024>(l16, sb, m0v); else if (n == 2) glds_s<2048>(l16, sb, m0v); else glds_s<3072>(l16, sb, m0v);
    } else {
      const int q = n - 4;
      glds_s<0>(boff[q >> 1], wsp + kk * 128 + (q & 1) * 64, dst + 16384 + (wave * 8 + (q & 1) * 4 + (q >> 1)) * 1024);
    }
  };
  f32x4 acc[4][8] = {};
#pragma unroll
  for (int n = 0; n < 12; ++n) piece(0, n);
#pragma unroll
  for (int n = 0; n < 12; ++n) piece(1, n);
  for (int kk = 0; kk < 32; ++kk) {
    if (kk + 1 < 32) vmwait<12>(); else vmwait<0>();
    lds_barrier();
    LAS char* b = lds + (kk % 3) * 49152 + lane * 16;
    bf16x8 bf[2][4], af[2][4];
#pragma unroll
    for (int Tt = 0; Tt < 4; ++Tt) bf[0][Tt] = *(LAS bf16x8*)(b + 16384 + (wave * 8 + Tt) * 1024);
#pragma unroll
    for (int q = 0; q < 4; ++q) af[0][q] = *(LAS bf16x8*)(b + q * 1024);
#pragma unroll
    for (int ch = 0; ch < 4; ++ch) {
      SB();
      if (ch < 3) {
#pragma unroll
        for (int q = 0; q < 4; ++q) af[(ch + 1) & 1][q] = *(LAS bf16x8*)(b + ((ch + 1) * 4 + q) * 1024);
      }
      if (ch == 0) {
#pragma unroll
        for (int Tt = 0; Tt < 4; ++Tt) bf[1][Tt] = *(LAS bf16x8*)(b + 16384 + (wave * 8 + 4 + Tt) * 1024);
      }
#pragma unroll
      for (int q = 0; q < 4; ++q) {
#pragma unroll
        for (int Tt = 0; Tt < 4; ++Tt) acc[Tt][(ch & 1) * 4 + q] = mfma16(af[ch & 1][q], bf[ch >> 1][Tt], acc[Tt][(ch & 1) * 4 + q]);
        if (q < 3) { SB(); if (kk + 2 < 32) piece(kk + 2, ch * 3 + q); SB(); }
      }
    }
    SB();
  }
  lds_barrier();
#pragma unroll 4
  for (int n = 0; n < 32; ++n) {
    const int e = wave * 32 + n;
    const int G = ((lane >> 2) - (e & 3)) & 15;
    glds16(p.w_out + (size_t)(fc * 128 + e) * DM + G * 32 + (lane & 3) * 8, lbase + e * 1024);
  }
  {
    const int rg = MAPV ? tp / 2 : tp / 3;
    wait_flags(rF, lane < 4 ? (unsigned)((F_Z1 + rg * 7 + fc) * 128 + lane * 4) : 0x80000000u, tp * 8 + 8, budget);
  }
  const rsrc_t rZ1 = mkrsrc(ws + OFF_Z1, NSEG * 2 * 28 * 1024);
  bf16x8 ub[4][4];
#pragma unroll
  for (int Tt = 0; Tt < 4; ++Tt) {
    const int sg = tp * 8 + wave * 2 + (Tt >> 1);
    const float bias = bflo((unsigned)p.b_sp[sg * 32 + 8 * (c >> 2) + 4 * (Tt & 1) + (c & 3)]);
#pragma unroll
    for (int m = 0; m < 4; ++m) {
      const u32x4 z = ld128(rZ1, lane * 16, ((sg * 2 + (Tt & 1)) * 28 + fc * 4 + m) * 1024, 16);
      const f32x4 g0 = acc[Tt][2 * m] + bias, g1 = acc[Tt][2 * m + 1] + bias;
      ub[Tt][m] = asb8((u32x4){pk2(bflo(z.x) * g0[0], bflo(z.y) * g0[1]), pk2(bflo(z.z) * g0[2], bflo(z.w) * g0[3]),
                               pk2(bfhi(z.x) * g1[0], bfhi(z.y) * g1[1]), pk2(bfhi(z.z) * g1[2], bfhi(z.w) * g1[3])});
    }
  }
  vmwait<0>();
  lds_barrier();
  const rsrc_t rS = mkrsrc(ws + OFF_SL, NSEG * NSLAB * 32768);
  for (int P16 = 0; P16 < 16; ++P16) {
    bf16x8 wf[2][4];
#pragma unroll
    for (int t = 0; t < 2; ++t)
#pragma unroll
      for (int m = 0; m < 4; ++m) {
        bf16x4 pc[2];
#pragma unroll
        for (int u = 0; u < 2; ++u) {
          const int e = 32 * m + 16 * u + 4 * g + (c >> 2);
          pc[u] = trrd(lds + e * 1024 + ((P16 + (e & 3)) & 15) * 64 + (c & 3) * 16 + t * 8);
        }
        wf[t][m] = cat8(pc[0], pc[1]);
      }
#pragma unroll
    for (int Tt = 0; Tt < 4; ++Tt) {
      f32x4 a0 = {}, a1 = {};
#pragma unroll
      for (int m = 0; m < 4; ++m) { a0 = mfma16(wf[0][m], ub[Tt][m], a0); a1 = mfma16(wf[1][m], ub[Tt][m], a1); }
      const int sg = tp * 8 + wave * 2 + (Tt >> 1);
      st128((u32x4){pk2(a0[0], a0[1]), pk2(a0[2], a0[3]), pk2(a1[0], a1[1]), pk2(a1[2], a1[3])}, rS, lane * 16, (sg * NSLAB + 32 + fc) * 32768 + ((Tt & 1) * 16 + P16) * 1024);
    }
  }
  vmwait<0>();
  put_flag(rF, (F_SP + tile) * 128 + wave * 4, 1, lane);
}

__device__ __forceinline__ float wavesum(float x) {
  x = rowsum16(x);
  return xsum_g(x);
}
__device__ __forceinline__ void ln_role(const Params& p, LAS char* lds, int i) {
  const int tid = threadIdx.x, lane = tid & 63, wave = __builtin_amdgcn_readfirstlane(tid >> 6);
  char* ws = p.ws;
  u32x4 v[8];
#pragma unroll
  for (int r = 0; r < 8; ++r) v[r] = *(const u32x4*)(p.x + (size_t)(i * 32 + wave * 8 + r) * DM + lane * 8);
  float f[8][8], m[8];
#pragma unroll
  for (int r = 0; r < 8; ++r) {
    f[r][0] = bflo(v[r].x); f[r][1] = bfhi(v[r].x); f[r][2] = bflo(v[r].y); f[r][3] = bfhi(v[r].y);
    f[r][4] = bflo(v[r].z); f[r][5] = bfhi(v[r].z); f[r][6] = bflo(v[r].w); f[r][7] = bfhi(v[r].w);
    m[r] = rowsum16(((f[r][0] + f[r][1]) + (f[r][2] + f[r][3])) + ((f[r][4] + f[r][5]) + (f[r][6] + f[r][7])));
  }
#pragma unroll
  for (int r = 0; r < 8; ++r) {
    const float mean = xsum_g(m[r]) * (1.f / DM);
    float q = 0.f;
#pragma unroll
    for (int j = 0; j < 8; ++j) { f[r][j] -= mean; q += f[r][j] * f[r][j]; }
    m[r] = rowsum16(q);
  }
#pragma unroll
  for (int r = 0; r < 8; ++r) {
    const float rs = 1.f / sqrtf(xsum_g(m[r]) * (1.f / DM) + 1e-5f);
    const int T = (r >> 2) & 1, ii = 4 * wave + (r & 3), ks = lane >> 2;
    *(LAS u32x4*)(lds + (T * 16 + ks) * 1024 + (((lane & 3) * 16 + ii + ks) & 63) * 16) =
        (u32x4){pk2(f[r][0] * rs, f[r][1] * rs), pk2(f[r][2] * rs, f[r][3] * rs), pk2(f[r][4] * rs, f[r][5] * rs), pk2(f[r][6] * rs, f[r][7] * rs)};
  }
  lds_barrier();
  const rsrc_t rN = mkrsrc(ws + OFF_XN + (size_t)i * 32768, 32768);
#pragma unroll
  for (int n = 0; n < 8; ++n) {
    const int fr = wave * 8 + n;
    st128(*(LAS u32x4*)(lds + fr * 1024 + ((lane + (fr & 15)) & 63) * 16), rN, lane * 16, fr * 1024);
  }
  vmwait<0>();
  lds_barrier();
  put_flag(mkrsrc(ws + OFF_FLG, FLG_BYTES), (F_XN + (i >> 5)) * 128 + (i & 31) * 4, 1, tid);
}
__device__ __forceinline__ void init_role(const Params& p) {
  const int tid = threadIdx.x;
  char* ws = p.ws;
  const rsrc_t rX = mkrsrc(ws + OFF_X, X_ZERO);
  if (tid >= 64) {
    for (int q = tid - 64; q < 512; q += 192) {
      u32x4 o;
#pragma unroll
      for (int j = 0; j < 4; ++j) {
        const int d = q * 4 + j;
        const int line = d >> 5, rp = (d >> 4) & 1, cc = (d >> 1) & 7, t0 = (d & 1) * 2;
        const int bb = line & 1, g = (line >> 1) & 3, sk = line >> 3;
        const int r = 2 * g + rp, k0 = 64 * sk + 16 * t0 + 8 * bb + cc;
        const unsigned v = (unsigned)p.mem0[r * DM + k0] | ((unsigned)p.mem0[r * DM + k0 + 16] << 16);
        o[j] = v > 1u ? v : 1u;
      }
      st128(o, rX, q * 16, X_M); st128(o, rX, q * 16, X_M + 8192);
    }
  } else if (tid == 0) {
    const char* base = ws + OFF_X + X_ZERO + 16384;
    unsigned best[2] = {~0u, ~0u};
    for (int r = 0; r < 5; ++r)
      for (int par = 0; par < 2; ++par) {
        const char* a = base + par * 8192 + 1024;
        const unsigned t0 = clk32();
        asm volatile("global_store_dword %0, %1, off sc1\n\ts_waitcnt vmcnt(0)" :: "v"(a), "v"(t0) : "memory");
        const unsigned t1 = clk32();
        best[par] = min(best[par], t1 - t0);
      }
    const unsigned xcc = (unsigned)__builtin_amdgcn_s_getreg(20 | (3 << 11)) & 0xfu;
    const unsigned sel = ((best[1] < best[0]) ? 1u : 0u) ^ ((xcc >> 2) & 1u);
    __builtin_amdgcn_raw_buffer_store_b128((u32x4){sel + 1u, best[0], best[1], xcc}, rX, OFF_FLG + F_SEL * 128, 0, 16);
  }
}

constexpr int L_MST = 0, L_KX = 8192, L_VX = 16384, L_QI = 24576, L_OI = 30976, L_WO = 32768, L_S1 = 98304, L_WV = 98304, L_KV1 = 147456;
constexpr int QI_G = 400, QI_W = 4 * QI_G;
#define ST(i)
#define CNT(i)
#define FT 0
#define RTC() ((unsigned)__builtin_amdgcn_s_memrealtime())
#define TRS(x)
#define TA 100
#define CA 2600
#define CB 3550
#define TB 120
#define D1 0
#define D2 0

__device__ __forceinline__ unsigned min3u(unsigned a, unsigned b, unsigned c) { return min(min(a, b), c); }
__device__ __forceinline__ unsigned min4u(u32x4 a) { return min(min3u(a.x, a.y, a.z), a.w); }

__device__ __forceinline__ void chain_role(const Params& p, LAS char* lds, int h, int a) {
  const int tid = threadIdx.x, lane = tid & 63, wave = __builtin_amdgcn_readfirstlane(tid >> 6);
  const int g = lane >> 4, c = lane & 15;
  char* ws = p.ws;
  const unsigned lbase = (unsigned)(uintptr_t)lds;

  bf16x16 wq[2][8], wk[2][8], wv[2][8];
  {
    auto issue = [&](const u16* W, int half, int soff) {
#pragma unroll 4
      for (int n = 0; n < 16; ++n) {
        const int row = (wave * 16 + n) * 4 + (lane >> 4);
        const int x = lane & 15;
        const int rot = (row & 3) + 4 * ((row >> 4) & 1);
        const int G = ((x >> 1) - rot) & 7;
        glds16(W + (size_t)(half * 256 + row) * (NH * DH) + h * DH + G * 16 + (x & 1) * 8, lbase + soff + (wave * 16 + n) * 1024);
      }
    };
    auto fetch = [&](bf16x16 (&w)[2][8], int half, int soff) {
#pragma unroll
      for (int m = 0; m < 4; ++m) {
        const int s4 = (wave + m) & 3;
#pragma unroll
        for (int t = 0; t < 2; ++t) {
          bf16x4 pc[4];
#pragma unroll
          for (int u = 0; u < 4; ++u) {
            const int q = c >> 2;
            const int row = 64 * s4 + 32 * (u & 1) + 16 * (q & 1) + 8 * (u >> 1) + 4 * (g >> 1) + 2 * (q >> 1) + (g & 1);
            const int rot = (row & 3) + 4 * ((row >> 4) & 1);
            pc[u] = trrd(lds + soff + row * 256 + ((wave * 2 + t + rot) & 7) * 32 + (c & 3) * 8);
          }
          w[t][2 * m + half] = cat16(cat8(pc[0], pc[1]), cat8(pc[2], pc[3]));
        }
      }
    };
    issue(p.Wq, 0, 0); issue(p.Wq, 1, L_S1);
    asm volatile("s_waitcnt vmcnt(16)" ::: "memory"); lds_barrier();
    fetch(wq, 0, 0); lds_barrier();
    issue(p.Wk, 0, 0);
    asm volatile("s_waitcnt vmcnt(16)" ::: "memory"); lds_barrier();
    fetch(wq, 1, L_S1); lds_barrier();
    issue(p.Wk, 1, L_S1);
    asm volatile("s_waitcnt vmcnt(16)" ::: "memory"); lds_barrier();
    fetch(wk, 0, 0); lds_barrier();
    issue(p.Wv, 0, 0);
    asm volatile("s_waitcnt vmcnt(16)" ::: "memory"); lds_barrier();
    fetch(wk, 1, L_S1); lds_barrier();
    issue(p.Wv, 1, L_S1);
    asm volatile("s_waitcnt vmcnt(16)" ::: "memory"); lds_barrier();
    fetch(wv, 0, 0); lds_barrier();
#pragma unroll 4
    for (int n = 0; n < 16; ++n) {
      const int e = (wave * 16 + n) * 2 + (lane >> 5);
      const int x = lane & 31;
      const int rot = (e & 1) + 2 * ((e >> 2) & 1) + 4 * ((e >> 3) & 1);
      const int G = ((x >> 1) - rot) & 15;
      glds16(p.Wo + (size_t)(h * DH + e) * DM + a * 256 + G * 16 + (x & 1) * 8, lbase + L_WO + (wave * 16 + n) * 1024);
    }
    asm volatile("s_waitcnt vmcnt(16)" ::: "memory"); lds_barrier();
    fetch(wv, 1, L_S1);
    asm volatile("s_waitcnt vmcnt(0)" ::: "memory"); lds_barrier();
    {
      bf16x8 wlo[8], whi[8];
#pragma unroll
      for (int n = 0; n < 8; ++n) {
        const int q = c >> 2, rot = (q & 1) + 2 * (g & 1) + 4 * (q >> 1);
        LAS char* b = lds + L_WO + (64 * (n >> 2) + 16 * (g >> 1) + 8 * (q >> 1) + 4 * (g & 1) + (q & 1)) * 512 + ((wave * 4 + (n & 3) + rot) & 15) * 32 + (c & 3) * 8;
        wlo[n] = cat8(trrd(b), trrd(b + 2 * 512)); whi[n] = cat8(trrd(b + 32 * 512), trrd(b + 34 * 512));
      }
      lds_barrier();
#pragma unroll
      for (int n = 0; n < 8; ++n) { LAS char* d = lds + L_WO + (wave * 8 + n) * 2048 + lane * 16; *(LAS bf16x8*)d = wlo[n]; *(LAS bf16x8*)(d + 1024) = whi[n]; }
    }
#pragma unroll
    for (int t = 0; t < 2; ++t)
#pragma unroll
      for (int u = 5; u < 8; ++u) {
        LAS char* d = lds + L_WV + (wave * 6 + (u == 5 ? 4 : (u - 6) * 2) + t) * 2048 + lane * 16;
        *(LAS bf16x8*)d = __builtin_shufflevector(wv[t][u], wv[t][u], 0, 1, 2, 3, 4, 5, 6, 7);
        *(LAS bf16x8*)(d + 1024) = __builtin_shufflevector(wv[t][u], wv[t][u], 8, 9, 10, 11, 12, 13, 14, 15);
      }
#pragma unroll
    for (int t = 0; t < 2; ++t)
#pragma unroll
      for (int u = 0; u < 8; ++u) {
        asm volatile("" : "+a"(wk[t][u]));
        if (u < 5) asm volatile("" : "+a"(wv[t][u])); else asm volatile("" : "+a"(wq[t][u]));
      }
  }

  const rsrc_t rX = mkrsrc(ws + OFF_X, X_ZERO);
  int spin_limit = SPIN_LIMIT;
  wait_flags(rX, (unsigned)(OFF_FLG + F_SEL * 128), 1, spin_limit);
  const unsigned sel = (__builtin_amdgcn_readfirstlane(__builtin_amdgcn_raw_buffer_load_b32(rX, (unsigned)(OFF_FLG + F_SEL * 128), 0, 16)) - 1u) & 1u;
  const rsrc_t rKVM = mkrsrc(ws + (a ? OFF_VM : OFF_KM) + (size_t)h * NSEG * 2048, NSEG * 2048);
  constexpr unsigned kvd = 256u;
  const int i16 = c, pr = (i16 >> 1) & 1;
  const int sidx = pr ? 0xEEEE : 0x4444;
  const unsigned vM = X_M + sel * 8192 + wave * 1024 + (i16 >> 2) * 256 + (g >> 1) * 128 + (i16 & 1) * 64 + (g & 1) * 32 + pr * 16;
  const int myline = ((a * 4 + wave) * 4 + g) * 2 + (c >> 3);
  const unsigned vPst = (myline >> 1) * 16384 + sel * 8192 + (myline & 1) * 4096 + h * 128 + (c & 7) * 16;
  const int rline = h * 2 + a;
  const unsigned vPld = (rline >> 1) * 16384 + sel * 8192 + (rline & 1) * 4096 + lane * 16;
  const unsigned vMst = ((g < 2) && (c < 8)) ? (unsigned)(X_M + sel * 8192 + rline * 128 + g * 64 + c * 8) : 0x80000000u;
  const bool lo8 = c < 8;
  const float CEXP = 0.08838834764831845f * 1.4426950408889634f;
  const unsigned vKV0 = a ? (unsigned)((2 * wave) * 256 + lane * 4) : (unsigned)(wave * 512 + (c >> 2) * 64 + g * 16 + (c & 3) * 4);
  int qi_st = L_QI + wave * QI_W + (c >> 2) * QI_G + (2 * g) * 16 + (c & 3) * 4;
  asm volatile("" : "+v"(qi_st));
  int qf_ld = L_QI + g * QI_G + (c & 7) * 16;
  asm volatile("" : "+v"(qf_ld));
  const int kf_ld = L_QI + g * QI_G + 256 + (2 * (c >> 2) + (c & 1)) * 16;
  int wo_ld = L_WO + wave * 16384 + lane * 16, wv_ld = L_WV + wave * 12288 + lane * 16;
  asm volatile("" : "+v"(wo_ld), "+v"(wv_ld));
  const int oi_st = L_MST + (wave >> 1) * 1024 + ((2 * (wave & 1) + (c >> 3)) * 16 + 4 * ((c & 7) >> 1) + 2 * (g >> 1) + (c & 1)) * 16 + (g & 1) * 8;

  const char* kbase = ws + OFF_KF + (size_t)h * NSEG * 8192;
  const char* kvb = kbase + wave * 2048;
  auto kv_go = [&](const char* b, const char* b2, unsigned d, unsigned l16) {
    asm volatile("s_mov_b32 m0, %3\n\ts_nop 0\n\tglobal_load_lds_dwordx4 %0, %1 sc1\n\tglobal_load_lds_dwordx4 %0, %1 offset:1024 sc1\n\ts_add_u32 m0, m0, 0x2000\n\ts_nop 0\n\t"
                 "global_load_lds_dwordx4 %0, %2 sc1\n\tglobal_load_lds_dwordx4 %0, %2 offset:1024 sc1" :: "v"(l16), "s"(b), "s"(b2), "s"(d) : "memory", "scc");
  };
  auto kv_dma = [&](int sg, unsigned l16) { kv_go(kvb + sg * 8192, kvb + sg * 8192 + (OFF_VF - OFF_KF), lbase + ((sg & 1) ? L_KV1 : L_KX) + wave * 2048, l16); };
  wait_flags(rX, lane < 4 ? (unsigned)(OFF_FLG + (F_KV + h) * 128 + lane * 4) : 0x80000000u, 2, spin_limit);
  kv_dma(0, lane * 16); kv_dma(1, lane * 16);
  vmwait<0>();
  int kvknown = 2;
  const unsigned l16c = lane * 16;
  int mst_st = L_MST + wave * 1024 + lane * 16, vx_ld = L_VX + wave * 2048 + lane * 16;
  asm volatile("" : "+v"(mst_st), "+v"(vx_ld));
  const unsigned vTS = wave == 3 ? (unsigned)(X_TS + sel * 8192 + lane * 128) : 0x80000000u;
  const unsigned vTSst = (lane == 0 && wave == 3) ? (unsigned)(X_TS + sel * 8192 + rline * 128) : 0x80000000u;
  const unsigned vPz = vPld + wave * 1024;
  const unsigned vCH = (tid == 64) ? (unsigned)(OFF_FLG + (F_CH + rline) * 128) : 0x80000000u;
  const unsigned vMz = wave == 3 ? vMst : 0x80000000u;
  if (wave == 3) {
    bf16x8 asel;
#pragma unroll
    for (int j = 0; j < 8; ++j) asel[j] = (__bf16)((c == j) ? 1.0f : 0.0f);
    *(LAS bf16x8*)(lds + L_OI + lane * 16) = asel;
    *(LAS int*)(lds + L_OI + 1028) = 2;
  }
  int mst_ld[3];
#pragma unroll
  for (int m = 1; m < 4; ++m) { mst_ld[m - 1] = L_MST + ((wave + m) & 3) * 1024 + lane * 16; asm volatile("" : "+v"(mst_ld[m - 1])); }

  f32x4 aq0 = zero4(), aq1 = zero4(), ak0 = zero4(), ak1 = zero4(), av0 = zero4(), av1 = zero4();
  lds_barrier();
  bf16x8 kxf[8];
#pragma unroll
  for (int n = 0; n < 8; ++n) kxf[n] = *(LAS bf16x8*)(lds + L_KX + n * 1024 + lane * 16);
  u32x4 f0 = ld128(rX, vM, 0, 16), f1 = ld128(rX, vM, 4096, 16);
  unsigned kvo0, kvo1;
  unsigned coff = clock_offset();
#define FENCE() asm volatile("" ::: "memory")
#define KEEP(x) asm volatile("" : "+v"(x))
#define Q0(u) do { if ((u) < 5) SMF_V(aq0, af[u], wq[0][u]); else SMF_A(aq0, af[u], wq[0][u]); } while (0)
#define Q1(u) do { if ((u) < 5) SMF_V(aq1, af[u], wq[1][u]); else SMF_A(aq1, af[u], wq[1][u]); } while (0)
#define QQ(u) do { Q0(u); Q1(u); } while (0)
#define K0(u) SMF_A(ak0, af[u], wk[0][u])
#define K1(u) SMF_A(ak1, af[u], wk[1][u])
#define KK(u) do { K0(u); K1(u); } while (0)
#define V0(u) SMF_A(av0, af[u], wv[0][u])
#define V1(u) SMF_A(av1, af[u], wv[1][u])
#define VV(u) do { V0(u); V1(u); } while (0)
#define LDA(u) do { FENCE(); af[u] = *(LAS bf16x8*)(lds + mst_ld[((u) >> 1) - 1] + ((u) & 1) * 4096); FENCE(); } while (0)
#define LDF(base, n) cat16(*(LAS bf16x8*)(lds + (base) + (n) * 2048), *(LAS bf16x8*)(lds + (base) + (n) * 2048 + 1024))
#define DPPMAX(x, ctl, rm) do { x = max(x, (int)__builtin_amdgcn_update_dpp((int)0x80000000, x, ctl, rm, 0xf, false)); KEEP(x); } while (0)
  for (int s = 0; s < NSEG; ++s) {
    const unsigned sM = (s & 3) * 16384;
    const bool kvnow = kvknown >= s + 3 || s + 2 >= NSEG;
    const char* dsrc = kvb + ((kvnow && s + 2 < NSEG) ? s + 2 : s) * 8192;
    const char* dsrc2 = dsrc + (OFF_VF - OFF_KF);
    unsigned ddst = lbase + ((s & 1) ? L_KV1 : L_KX) + wave * 2048, so0 = s * 2048, so1 = s * 2048 + kvd;
    asm volatile("" : "+s"(dsrc), "+s"(dsrc2), "+s"(ddst), "+s"(so0), "+s"(so1) :: "memory");
    bf16x8 af[8];
    af[0] = asb8(f0); af[1] = asb8(f1);
    *(LAS u32x4*)(lds + mst_st) = f0;
    *(LAS u32x4*)(lds + mst_st + 4096) = f1;
    FENCE();
    Q0(0);
    unsigned mn = min3u(f0.x, f0.y, f0.z); KEEP(mn);
    __builtin_amdgcn_raw_buffer_store_b128(__builtin_bit_cast(u32x4, av0), rX, vPz, ((s + 1) & 1) * 524288, 16);
    FENCE();
    Q1(0);
    mn = min3u(mn, f0.w, f1.x); KEEP(mn);
    __builtin_amdgcn_raw_buffer_store_b64(__builtin_bit_cast(u32x2, __builtin_shufflevector(av0, av0, 0, 1)), rX, vMz, ((s + 2) & 3) * 16384, 16);
    FENCE();
    Q0(1);
    mn = min(min3u(mn, f1.y, f1.z), f1.w); KEEP(mn);
    Q1(1);
    __builtin_amdgcn_raw_buffer_store_b32((unsigned)s, rX, vCH, 0, 16);
    FENCE();
    if (__builtin_expect(__builtin_amdgcn_ballot_w64(mn == 0) != 0, 0)) {
      for (int spin = 0; spin < spin_limit; ++spin) {
        reld2(rX, vM, sM, sM + 4096, f0, f1);
        if (__builtin_amdgcn_ballot_w64(min(min4u(f0), min4u(f1)) == 0) == 0) break;
        if (spin == spin_limit - 1) spin_limit = 0;
      }
      af[0] = asb8(f0); af[1] = asb8(f1);
      *(LAS u32x4*)(lds + mst_st) = f0;
      *(LAS u32x4*)(lds + mst_st + 4096) = f1;
      aq0 = zero4(); aq1 = zero4();
      asm volatile("s_nop 1" : "+v"(aq0), "+v"(aq1), "+v"(af[0]), "+v"(af[1]));
      QQ(0); QQ(1);
    }
    lds_barrier();
    unsigned long long mt64;
    asm volatile("s_memtime %0" : "=s"(mt64) :: "memory");
    TRS(tb1);
    ST(1);
    bf16x16 wl[6];
    LDA(2); LDA(3); K0(0); LDA(4); LDA(5); K1(0); LDA(6); LDA(7); K0(1); K1(1);
    VV(0); V0(1);
    asm volatile("s_waitcnt lgkmcnt(0)" : "+s"(mt64) :: "memory");
    const unsigned mb1 = (unsigned)mt64;
    const unsigned rown = mb1 + coff;
    __builtin_amdgcn_raw_buffer_store_b32(rown, rX, vTSst, (s & 3) * 16384, 16);
    FENCE();
    V1(1);
    QQ(2); FENCE(); wl[0] = LDF(wv_ld, 0); FENCE();
    QQ(3); FENCE(); wl[1] = LDF(wv_ld, 1); FENCE();
    QQ(4); FENCE(); wl[2] = LDF(wv_ld, 2); FENCE();
    QQ(5); FENCE(); wl[3] = LDF(wv_ld, 3); FENCE();
    QQ(6); QQ(7);
    K0(2); K1(2); K0(3);
    asm volatile("s_nop 0" : "+v"(aq0), "+v"(aq1));
    {
      float t0 = aq0[0] + aq0[2], t1 = aq1[0] + aq1[2]; KEEP(t0); KEEP(t1);
      K1(3);
      float t2 = aq0[1] + aq0[3], t3 = aq1[1] + aq1[3]; KEEP(t2); KEEP(t3);
      K0(4);
      const unsigned d0 = pk2(t0, t1), d1 = pk2(t2, t3);
      *(LAS unsigned*)(lds + qi_st) = d0; *(LAS unsigned*)(lds + qi_st + 16) = d1;
      FENCE();
    }
    K1(4); K0(5); K1(5); K0(6);
    lds_barrier();
    TRS(tb2);
    ST(2);
    bf16x8 qf[4], kf[4];
    qf[0] = *(LAS bf16x8*)(lds + qf_ld); qf[1] = *(LAS bf16x8*)(lds + qf_ld + QI_W); FENCE();
    K1(6); FENCE();
    qf[2] = *(LAS bf16x8*)(lds + qf_ld + 2 * QI_W); qf[3] = *(LAS bf16x8*)(lds + qf_ld + 3 * QI_W); FENCE();
    K0(7); FENCE(); wl[4] = LDF(wv_ld, 4); FENCE();
    K1(7); FENCE(); wl[5] = LDF(wv_ld, 5); FENCE();
    V0(2); V1(2); V0(3);
    asm volatile("" : "+v"(ak0), "+v"(ak1));
    unsigned kd0, kd1;
    f32x4 sx0, sx1, sm;
    {
      float t0 = ak0[0] + ak0[2], t1 = ak1[0] + ak1[2]; KEEP(t0); KEEP(t1);
      V1(3);
      float t2 = ak0[1] + ak0[3], t3 = ak1[1] + ak1[3]; KEEP(t2); KEEP(t3);
      MF0m(sx0, kxf[0], qf[0]);
      kd0 = pk2(t0, t1); kd1 = pk2(t2, t3);
      *(LAS unsigned*)(lds + qi_st + 256) = kd0; *(LAS unsigned*)(lds + qi_st + 256 + 16) = kd1;
    }
    MF0m(sx1, kxf[1], qf[0]);
    MFm(sx0, kxf[2], qf[1]);
    int ts = (int)__builtin_amdgcn_raw_buffer_load_b32(rX, vTS, (s & 3) * 16384, 16);
    MFm(sx1, kxf[3], qf[1]);
    MFm(sx0, kxf[4], qf[2]); MFm(sx1, kxf[5], qf[2]);
    lds_barrier();
    TRS(tb3);
    ST(3);
    kf[0] = *(LAS bf16x8*)(lds + kf_ld); kf[1] = *(LAS bf16x8*)(lds + kf_ld + QI_W);
    MFm(sx0, kxf[6], qf[3]);
    kf[2] = *(LAS bf16x8*)(lds + kf_ld + 2 * QI_W); kf[3] = *(LAS bf16x8*)(lds + kf_ld + 3 * QI_W);
    MFm(sx1, kxf[7], qf[3]);
    const bf16x8 vx0 = *(LAS bf16x8*)(lds + vx_ld), vx1 = *(LAS bf16x8*)(lds + vx_ld + 1024);
    FENCE();
    bf16x16 wo[8];
    SMF_V(av0, af[6], wl[0]); SMF_V(av1, af[6], wl[1]); FENCE();
    wo[0] = LDF(wo_ld, 0);
    MF0m(sm, kf[0], qf[0]);
    wo[1] = LDF(wo_ld, 1);
    MFm(sm, kf[1], qf[1]);
    float mm = max3f(sx0[0], sx0[1], sx0[2]), m2 = max3f(sx0[3], sx1[0], sx1[1]); KEEP(mm); KEEP(m2);
    MFm(sm, kf[2], qf[2]);
    m2 = max3f(m2, sx1[2], sx1[3]); mm = max2f(mm, m2); KEEP(mm);
    MFm(sm, kf[3], qf[3]);
    u32x2 on_lo, on_hi;
    asm volatile("v_mov_b64 %0, %2\n\tv_mov_b64 %1, %2" : "=v"(on_lo), "=v"(on_hi) : "s"(0x3f803f803f803f80ull));
    const bf16x8 ones = asb8(__builtin_shufflevector(on_lo, on_hi, 0, 1, 2, 3));
    wo[4] = LDF(wo_ld, 4); FENCE();
    SMF_V(av0, af[7], wl[2]); FENCE(); wo[5] = LDF(wo_ld, 5); FENCE(); SMF_V(av1, af[7], wl[3]);
    SMF_V(av0, af[5], wl[4]);
    asm volatile("" : "+v"(sm));
    mm = max3f(mm, sm[0], sm[1]);
    { float t_;
      asm volatile("v_mov_b32 %1, %0\n\tv_smfmac_f32_16x16x64_bf16 %2, %4, %5, %8\n\ts_nop 0\n\tv_permlane32_swap_b32 %0, %1\n\tv_max_f32 %0, %0, %1\n\tv_mov_b32 %1, %0\n\t"
                   "v_smfmac_f32_16x16x64_bf16 %3, %6, %7, %8\n\ts_nop 0\n\tv_permlane16_swap_b32 %0, %1\n\tv_max_f32 %0, %0, %1"
                   : "+v"(mm), "=&v"(t_), "+v"(av1), "+v"(av0) : "v"(af[5]), "v"(wl[5]), "v"(af[4]), "a"(wv[0][4]), "v"(sidx)); }
    V1(4);
    float mc = mm * CEXP; KEEP(mc);
    float ex[8];
#pragma unroll
    for (int j = 0; j < 4; ++j) { ex[j] = __builtin_amdgcn_exp2f(sx0[j] * CEXP - mc); ex[4 + j] = __builtin_amdgcn_exp2f(sx1[j] * CEXP - mc); }
    const u32x4 exp_ = {pk2(ex[0], ex[1]), pk2(ex[2], ex[3]), pk2(ex[4], ex[5]), pk2(ex[6], ex[7])};
    f32x4 ax, dn;
    u32x2 vv;
    { const bf16x8 b0 = asb8((u32x4){lo8 ? exp_.x : 0u, lo8 ? exp_.y : 0u, lo8 ? exp_.z : 0u, lo8 ? exp_.w : 0u});
      const bf16x8 ea = asb8(exp_);
      MFN0(ax, vx0, b0);
      float e0 = __builtin_amdgcn_exp2f(sm[0] * CEXP - mc); KEEP(e0);
      MF0(dn, ones, ea);
      float e1 = __builtin_amdgcn_exp2f(sm[1] * CEXP - mc); KEEP(e1);
      const bf16x8 b1 = asb8((u32x4){lo8 ? 0u : exp_.x, lo8 ? 0u : exp_.y, lo8 ? 0u : exp_.z, lo8 ? 0u : exp_.w});
      MFN(ax, vx1, b1);
      asm volatile("" : "+v"(av0), "+v"(av1));
      const unsigned em = pk2(e0, e1);
      vv = (u32x2){pk2(av0[0] + av0[2], av0[1] + av0[3]), pk2(av1[0] + av1[2], av1[1] + av1[3])};
      const u32x2 bm = {lo8 ? em : 0u, lo8 ? 0u : em};
      const bf16x4 on2 = __builtin_shufflevector(ones, ones, 0, 1, 2, 3);
      asm volatile("s_nop 1\n\tv_mfma_f32_16x16x16_bf16 %0, %2, %4, %0\n\tv_mfma_f32_16x16x16_bf16 %1, %3, %4, %1" : "+v"(dn), "+v"(ax) : "v"(on2), "v"(vv), "v"(bm) : "memory"); }
    f32x4 ap0 = zero4(), ap1 = zero4(), ap2 = zero4();
    asm volatile("s_nop 0" : "+v"(dn), "+v"(ax) :: "memory");
    const float rinv = __builtin_amdgcn_rcpf(dn[0]);
    {
      const u32x2 o = {pk2(ax[0] * rinv, ax[1] * rinv), pk2(ax[2] * rinv, ax[3] * rinv)};
      *(LAS u32x2*)(lds + oi_st) = o;
    }
    FENCE();
    kvo0 = a ? vv.x : kd0; kvo1 = a ? vv.y : kd1;
    if (s == NSEG - 1) break;
    asm volatile("" : "+v"(ts) :: "memory");
    __builtin_amdgcn_raw_buffer_store_b32(kvo0, rKVM, vKV0, so0, 16);
    __builtin_amdgcn_raw_buffer_store_b32(kvo1, rKVM, vKV0, so1, 16);
    kv_go(dsrc, dsrc2, ddst, l16c);
    f32x4 ap3 = zero4();
    lds_barrier();
    TRS(tb4);
    const bf16x8 o0 = *(LAS bf16x8*)(lds + L_MST + lane * 16), o1 = *(LAS bf16x8*)(lds + L_MST + 1024 + lane * 16);
    FENCE();
    wo[2] = LDF(wo_ld, 2); wo[3] = LDF(wo_ld, 3); wo[6] = LDF(wo_ld, 6); wo[7] = LDF(wo_ld, 7);
    FENCE();
    SMF_V(ap0, o0, wo[0]); SMF_V(ap1, o0, wo[1]); SMF_V(ap0, o1, wo[4]); SMF_V(ap1, o1, wo[5]);
    SMF_V(ap2, o0, wo[2]); SMF_V(ap3, o0, wo[3]); SMF_V(ap2, o1, wo[6]);
    asm volatile("" : "+v"(ap0), "+v"(ap1));
    u32x4 pv;
    { float t0 = ap0[0] + ap0[2], t1 = ap1[0] + ap1[2]; KEEP(t0); KEEP(t1);
      SMF_V(ap3, o1, wo[7]);
      pv.x = max(pk2(t0, t1), 1u); pv.z = max(pk2(ap0[1] + ap0[3], ap1[1] + ap1[3]), 1u); }
    asm volatile("s_nop 3" : "+v"(pv.x), "+v"(pv.z), "+v"(ap2), "+v"(ap3));
    pv.y = max(pk2(ap2[0] + ap2[2], ap3[0] + ap3[2]), 1u); pv.w = max(pk2(ap2[1] + ap2[3], ap3[1] + ap3[3]), 1u);
    const unsigned sP = (s & 1) * 524288;
    st128(pv, rX, vPst, sP);
    ST(5);
    if (wave == 3) {
      asm volatile("v_subrev_u32 %0, %1, %0\n\tv_min_u32 %0, 0x800, %0\n\tv_and_b32 %0, 0x7ff, %0" : "+v"(ts) : "s"(rown));
      DPPMAX(ts, 0x111, 0xf); DPPMAX(ts, 0x112, 0xf); DPPMAX(ts, 0x114, 0xf); DPPMAX(ts, 0x118, 0xf); DPPMAX(ts, 0x142, 0xa); DPPMAX(ts, 0x143, 0xc);
      const int tsm = __builtin_amdgcn_readlane(ts, 63);
      const unsigned tbase = mb1 + (unsigned)tsm;
      const bf16x8 asel = *(LAS bf16x8*)(lds + L_OI + lane * 16);
      FENCE();
      if (D1) __builtin_amdgcn_s_sleep(D1);
      wait_until(tbase + CA);
      u32x4 r0 = ld128(rX, vPld, sP, 16), r1 = ld128(rX, vPld, sP + 1024, 16), r2 = ld128(rX, vPld, sP + 2048, 16), r3 = ld128(rX, vPld, sP + 3072, 16);
      f32x4 d0;
      unsigned rm;
      { const bf16x8 b0 = asb8(r0), b1 = asb8(r1), b2 = asb8(r2), b3 = asb8(r3);
        MF0(d0, asel, b0); rm = min4u(r0); KEEP(rm);
        MF(d0, asel, b1); rm = min3u(min3u(rm, r1.x, r1.y), r1.z, r1.w); KEEP(rm);
        MF(d0, asel, b2); rm = min3u(min3u(rm, r2.x, r2.y), r2.z, r2.w); KEEP(rm);
        MF(d0, asel, b3); rm = min3u(min3u(rm, r3.x, r3.y), r3.z, r3.w); KEEP(rm); }
      if (__builtin_expect(__builtin_amdgcn_ballot_w64(rm == 0) != 0, 0)) {
        for (int spin = 0; spin < spin_limit; ++spin) {
          reld4(rX, vPld, sP, r0, r1, r2, r3);
          if (__builtin_amdgcn_ballot_w64(min(min3u(min4u(r0), min4u(r1), min4u(r2)), min4u(r3)) == 0) == 0) break;
          if (spin == spin_limit - 1) spin_limit = 0;
        }
        const bf16x8 b0 = asb8(r0), b1 = asb8(r1), b2 = asb8(r2), b3 = asb8(r3);
        MFN0(d0, asel, b0); MF(d0, asel, b1); MF(d0, asel, b2); MF(d0, asel, b3);
        PIN1(7, d0);
      }
      PIN1(3, d0);
#pragma unroll
      for (int j = 0; j < 4; ++j) {
        float dj = d0[j];
        asm("v_add_f32_dpp %0, %0, %0 row_ror:8 row_mask:0xf bank_mask:0xf" : "+v"(dj));
        d0[j] = dj;
      }
      u32x2 mo = {max(pk2(d0[0], d0[1]), 1u), max(pk2(d0[2], d0[3]), 1u)};
      __builtin_amdgcn_raw_buffer_store_b64(mo, rX, vMst, ((s + 1) & 3) * 16384, 16);
      FENCE();
      ST(7);
      wait_until(tbase + CB);
    } else {
      if (wave == 0 && kvknown < s + 6 && kvknown < NSEG) {
        const int v = (int)__builtin_amdgcn_raw_buffer_load_b32(rX, (unsigned)(OFF_FLG + (F_KV + h) * 128) + ((l16c >> 2) & 12u), 0, 16);
        const int k_ = min(min(__builtin_amdgcn_readlane(v, 0), __builtin_amdgcn_readlane(v, 1)), min(__builtin_amdgcn_readlane(v, 2), __builtin_amdgcn_readlane(v, 3)));
        *(LAS int*)(lds + L_OI + 1028) = k_;
      }
      if (wave == 1) { const unsigned o_ = clock_offset(); *(LAS unsigned*)(lds + L_OI + 1024) = o_; }
      vmwait<5>();
    }
    if (__builtin_expect(!kvnow, 0)) {
      wait_flags(rX, l16c < 64 ? (unsigned)(OFF_FLG + (F_KV + h) * 128) + (l16c >> 2) : 0x80000000u, s + 3, spin_limit);
      kv_dma(s + 2, l16c);
    }
    lds_barrier();
    f0 = ld128(rX, vM, ((s + 1) & 3) * 16384, 16); f1 = ld128(rX, vM, ((s + 1) & 3) * 16384 + 4096, 16);
    FENCE();
    ST(8);
    aq0 = zero4(); aq1 = zero4(); ak0 = zero4(); ak1 = zero4(); av0 = zero4(); av1 = zero4();
#pragma unroll
    for (int n = 0; n < 8; ++n) kxf[n] = *(LAS bf16x8*)(lds + ((s & 1) ? L_KX : L_KV1) + n * 1024 + lane * 16);
    vx_ld += (s & 1) ? (L_KX - L_KV1) : (L_KV1 - L_KX);
    { const u32x2 cv = *(LAS u32x2*)(lds + L_OI + 1024); coff = __builtin_amdgcn_readfirstlane(cv.x); kvknown = __builtin_amdgcn_readfirstlane(cv.y); }
    FENCE();
  }
  __builtin_amdgcn_raw_buffer_store_b32(kvo0, rKVM, vKV0, (NSEG - 1) * 2048, 16);
  __builtin_amdgcn_raw_buffer_store_b32(kvo1, rKVM, vKV0, (NSEG - 1) * 2048 + kvd, 16);
  vmwait<0>();
  lds_barrier();
  __builtin_amdgcn_raw_buffer_store_b32((unsigned)NSEG, rX, vCH, 0, 16);
}

#define SD1 3
#define SD2 5
__device__ __forceinline__ void start_delay(int us) { for (int i = 0; i < us; ++i) __builtin_amdgcn_s_sleep(37); }
__global__ void __launch_bounds__(256, 1) k_main(Params p) {
  extern __shared__ __attribute__((aligned(16))) char smem[];
  LAS char* lds = (LAS char*)smem;
  const int b = blockIdx.x, x = b & 7, slot = b >> 3;
  int qi = -1;
  if (x < 4) {
    if (slot < 16) chain_role(p, lds, x * 8 + (slot >> 1), slot & 1);
    else if (slot < 18) qi = 8 + x * 2 + slot - 16;
    else if (b == 31 * 8 + 3) init_role(p);
    else {
      const int i = (slot - 18) * 4 + x;
      ln_role(p, lds, i);
      if (i < 9) ln_role(p, lds, 55 + i);
    }
  } else {
    const int y = x - 4;
    if (slot < 8) proj_role<0, 4>(p, lds, y * 8 + slot, 0, NSEG, F_KV + y * 8 + slot);
    else if (slot < 16) {
      const int i = y * 8 + slot - 8;
      start_delay(SD1);
      if (i < 28) proj_role<2, 2>(p, lds, i % 7, (i / 7) * 16, (i / 7) * 16 + 16, F_Z1 + i);
      ah_role(p, lds, i);
    } else if (slot < 30) {
      start_delay(SD1);
      sp_role(p, lds, y * 14 + slot - 16);
      lds_barrier();
      if (slot < 24) red_role(p, lds, y * 8 + slot - 16);
    } else qi = y * 2 + slot - 30;
  }
  if (qi >= 0) start_delay(SD2);
  if (qi >= 0) proj_role<1, 4>(p, lds, qi, 0, NSEG, F_Q + qi);
}

extern "C" void kernel_launch(void* const* d_in, const int* in_sizes, int n_in, void* d_out, int out_size, void* d_ws, size_t ws_size,
                              hipStream_t stream) {
  Params p;
  p.x = (const u16*)d_in[0]; p.mem0 = (const u16*)d_in[1]; p.Wq = (const u16*)d_in[2]; p.Wk = (const u16*)d_in[3];
  p.Wv = (const u16*)d_in[4]; p.Wo = (const u16*)d_in[5]; p.w_in = (const u16*)d_in[8]; p.w_sp = (const u16*)d_in[9];
  p.b_sp = (const u16*)d_in[10]; p.w_out = (const u16*)d_in[11]; p.out = (u16*)d_out;
  p.ws = (char*)(((uintptr_t)d_ws + (2u << 20) - 1) & ~(uintptr_t)((2u << 20) - 1));
  (void)hipFuncSetAttribute((const void*)k_main, hipFuncAttributeMaxDynamicSharedMemorySize, 163840);
  (void)hipMemsetAsync(p.ws + OFF_X, 0, X_ZERO, stream);
  hipLaunchKernelGGL(k_main, dim3(256), dim3(256), 163840, stream, p);
}
```

```cpp
#include <hip/hip_runtime.h>
#include <stdint.h>
#include <type_traits>

typedef __bf16 bf16x2 __attribute__((ext_vector_type(2)));
typedef __bf16 bf16x4 __attribute__((ext_vector_type(4)));
typedef __bf16 bf16x8 __attribute__((ext_vector_type(8)));
typedef __bf16 bf16x16 __attribute__((ext_vector_type(16)));
typedef float f32x4 __attribute__((ext_vector_type(4)));
typedef float f32x2 __attribute__((ext_vector_type(2)));
typedef unsigned u32x4 __attribute__((ext_vector_type(4)));
typedef unsigned u32x2 __attribute__((ext_vector_type(2)));
typedef unsigned short u16;
#define LAS __attribute__((address_space(3)))
#define GAS __attribute__((address_space(1)))
typedef __amdgpu_buffer_rsrc_t rsrc_t;

constexpr int S_ = 2048, DM = 512, NH = 32, DH = 128, NSEG = 64, FH = 896;
constexpr size_t OFF_X = 0;
constexpr size_t X_M = 1u << 20;
constexpr size_t X_TS = X_M + 65536;
constexpr size_t OFF_FLG = X_M + 131072;
constexpr size_t X_ZERO = OFF_FLG + 65536;
constexpr size_t OFF_XN = 4u << 20;
constexpr size_t OFF_KM = 8u << 20;
constexpr size_t OFF_VM = 16u << 20;
constexpr size_t OFF_Z1 = 24u << 20;
constexpr size_t OFF_Z2 = 32u << 20;
constexpr size_t OFF_ZN = 40u << 20;
constexpr size_t OFF_Y = 48u << 20;
constexpr size_t OFF_CELL = 80u << 20;
__device__ __forceinline__ unsigned blk(unsigned i, unsigned fs) { return i * 16384u + (((i >> 7) ^ fs) & 1u) * 8192u; }
constexpr int FLG_BYTES = 65536;
constexpr int F_KV = 2, F_Q = 34, F_CH = 72, F_AH = 136, F_LN = 168, F_SP = 232, F_Z2 = 288, F_Z1 = 344, F_XN = 376, F_SEL = 408, F_AH2 = 416;
#define S0SW 44
constexpr int S_SW = S0SW;
constexpr int SPIN_LIMIT = 200000;

__device__ __forceinline__ rsrc_t mkrsrc(const void* p, unsigned bytes) { return __builtin_amdgcn_make_buffer_rsrc((void*)p, 0, bytes, 0x00020000); }
__device__ __forceinline__ u32x4 ld128(rsrc_t r, unsigned v, unsigned s, int aux) {
  return aux ? __builtin_amdgcn_raw_buffer_load_b128(r, v, s, 16) : __builtin_amdgcn_raw_buffer_load_b128(r, v, s, 0);
}
__device__ __forceinline__ void st128(u32x4 v, rsrc_t r, unsigned vo, unsigned so) {
  __builtin_amdgcn_raw_buffer_store_b128(v, r, vo, so, 16);
  asm volatile("s_nop 1" :: "v"(v));
}
__device__ __forceinline__ void reld2(rsrc_t r, unsigned v, unsigned s0, unsigned s1, u32x4& a, u32x4& b) {
  asm volatile("s_nop 4\n\tbuffer_load_dwordx4 %0, %2, %3, %4 offen sc1\n\tbuffer_load_dwordx4 %1, %2, %3, %5 offen sc1\n\ts_waitcnt vmcnt(0)"
               : "=&v"(a), "=&v"(b) : "v"(v), "s"(r), "s"(s0), "s"(s1) : "memory");
}
__device__ __forceinline__ void reld4(rsrc_t r, unsigned v, unsigned s0, u32x4& a, u32x4& b, u32x4& c, u32x4& d) {
  asm volatile("s_nop 4\n\tbuffer_load_dwordx4 %0, %4, %5, %6 offen sc1\n\tbuffer_load_dwordx4 %1, %4, %5, %6 offen offset:1024 sc1\n\t"
               "buffer_load_dwordx4 %2, %4, %5, %6 offen offset:2048 sc1\n\tbuffer_load_dwordx4 %3, %4, %5, %6 offen offset:3072 sc1\n\ts_waitcnt vmcnt(0)"
               : "=&v"(a), "=&v"(b), "=&v"(c), "=&v"(d) : "v"(v), "s"(r), "s"(s0) : "memory");
}
__device__ __forceinline__ unsigned pk2(float a, float b) {
  bf16x2 v = {(__bf16)a, (__bf16)b};
  return __builtin_bit_cast(unsigned, v);
}
__device__ __forceinline__ f32x2 fold(f32x4 a) {
  f32x2 r;
  asm("v_pk_add_f32 %0, %1, %2" : "=v"(r) : "v"(__builtin_shufflevector(a, a, 0, 1)), "v"(__builtin_shufflevector(a, a, 2, 3)));
  return r;
}
__device__ __forceinline__ float bflo(unsigned u) { return __builtin_bit_cast(float, u << 16); }
__device__ __forceinline__ float bfhi(unsigned u) { return __builtin_bit_cast(float, u & 0xffff0000u); }
__device__ __forceinline__ void lds_barrier() { asm volatile("s_waitcnt lgkmcnt(0)\n\ts_barrier" ::: "memory"); }
__device__ __forceinline__ bf16x4 trrd(LAS char* p) { return __builtin_amdgcn_ds_read_tr16_b64_v4bf16((LAS bf16x4*)p); }
__device__ __forceinline__ bf16x8 cat8(bf16x4 a, bf16x4 b) { return __builtin_shufflevector(a, b, 0, 1, 2, 3, 4, 5, 6, 7); }
__device__ __forceinline__ bf16x16 cat16(bf16x8 a, bf16x8 b) { return __builtin_shufflevector(a, b, 0, 1, 2, 3, 4, 5, 6, 7, 8, 9, 10, 11, 12, 13, 14, 15); }
__device__ __forceinline__ bf16x8 asb8(u32x4 v) { return __builtin_bit_cast(bf16x8, v); }
__device__ __forceinline__ f32x4 mfma16(bf16x8 a, bf16x8 b, f32x4 c) { return __builtin_amdgcn_mfma_f32_16x16x32_bf16(a, b, c, 0, 0, 0); }
__device__ __forceinline__ f32x4 smfma(bf16x8 a, bf16x16 b, f32x4 c, int idx) { return __builtin_amdgcn_smfmac_f32_16x16x64_bf16(a, b, c, idx, 0, 0); }
__device__ __forceinline__ void glds16(const void* g, unsigned ldsoff) {
  unsigned keep;
  asm volatile("s_mov_b32 %0, m0\n\ts_mov_b32 m0, %2\n\ts_nop 0\n\tglobal_load_lds_dwordx4 %1, off\n\ts_mov_b32 m0, %0" : "=&s"(keep) : "v"(g), "s"(ldsoff) : "memory");
}
template <int IMM> __device__ __forceinline__ void glds_s(unsigned voff, const void* sbase, unsigned m0v) {
  asm volatile("s_mov_b32 m0, %2\n\ts_nop 0\n\tglobal_load_lds_dwordx4 %0, %1 offset:%3" :: "v"(voff), "s"(sbase), "s"(m0v), "i"(IMM));
}
template <int IMM> __device__ __forceinline__ void glds_c(unsigned voff, const void* sbase, unsigned m0v) {
  asm volatile("s_mov_b32 m0, %2\n\ts_nop 0\n\tglobal_load_lds_dwordx4 %0, %1 offset:%3 sc1" :: "v"(voff), "s"(sbase), "s"(m0v), "i"(IMM) : "memory");
}
template <int IMM> __device__ __forceinline__ void glds_d(unsigned voff, const void* sbase, unsigned m0v) {
  asm volatile("s_mov_b32 m0, %2\n\ts_nop 0\n\tglobal_load_lds_dwordx4 %0, %1 offset:%3 sc1" :: "v"(voff), "s"(sbase), "s"(m0v), "i"(IMM));
}
#define SB() __builtin_amdgcn_sched_barrier(0)
#define SMF_A(acc, a, b) asm volatile("v_smfmac_f32_16x16x64_bf16 %0, %1, %2, %3" : "+v"(acc) : "v"(a), "a"(b), "v"(sidx))
#define SMF_V(acc, a, b) asm volatile("v_smfmac_f32_16x16x64_bf16 %0, %1, %2, %3" : "+v"(acc) : "v"(a), "v"(b), "v"(sidx))
#define MF(acc, a, b) asm volatile("v_mfma_f32_16x16x32_bf16 %0, %1, %2, %0" : "+v"(acc) : "v"(a), "v"(b))
#define MF0(acc, a, b) asm volatile("v_mfma_f32_16x16x32_bf16 %0, %1, %2, 0" : "=&v"(acc) : "v"(a), "v"(b))
#define MFN(acc, a, b) asm volatile("s_nop 1\n\tv_mfma_f32_16x16x32_bf16 %0, %1, %2, %0" : "+v"(acc) : "v"(a), "v"(b))
#define MFN0(acc, a, b) asm volatile("s_nop 1\n\tv_mfma_f32_16x16x32_bf16 %0, %1, %2, 0" : "=&v"(acc) : "v"(a), "v"(b))
#define MFm(acc, a, b) asm volatile("v_mfma_f32_16x16x32_bf16 %0, %1, %2, %0" : "+v"(acc) : "v"(a), "v"(b) : "memory")
#define MF0m(acc, a, b) asm volatile("v_mfma_f32_16x16x32_bf16 %0, %1, %2, 0" : "=&v"(acc) : "v"(a), "v"(b) : "memory")
__device__ __forceinline__ f32x4 zero4() {
  f32x2 lo, hi;
  asm volatile("v_mov_b64 %0, 0\n\tv_mov_b64 %1, 0" : "=v"(lo), "=v"(hi));
  return __builtin_shufflevector(lo, hi, 0, 1, 2, 3);
}
#define PIN1(n, x) asm volatile("s_nop " #n : "+v"(x))
#define PIN2(n, x, y) asm volatile("s_nop " #n : "+v"(x), "+v"(y))
#define PIN4(n, x, y, z, w) asm volatile("s_nop " #n : "+v"(x), "+v"(y), "+v"(z), "+v"(w))
__device__ __forceinline__ unsigned clk32() { unsigned long long t; asm volatile("s_memtime %0\n\ts_waitcnt lgkmcnt(0)" : "=s"(t) :: "memory"); return (unsigned)t; }
__device__ __forceinline__ void wait_until(unsigned tgt) {
  asm volatile("s_memtime vcc\n\ts_waitcnt lgkmcnt(0)\n\ts_sub_i32 %0, %0, vcc_lo\n\ts_sub_i32 %0, %0, 60\n\ts_cmp_lt_i32 %0, 0\n\ts_cbranch_scc1 2f\n\t"
               "s_min_i32 %0, %0, 0x2000\n\ts_mul_i32 %0, %0, 585\n\ts_lshr_b32 %0, %0, 14\n\t.p2align 5\n1:\n\ts_sub_u32 %0, %0, 1\n\ts_cbranch_scc0 1b\n2:"
               : "+s"(tgt) :: "scc", "vcc", "memory");
}
__device__ __forceinline__ unsigned clock_offset() {
  unsigned long long r0, r1, r2, r3, r4, r5, r6, r7, r8, r9, m0;
  asm volatile("s_memtime %10\n\ts_memrealtime %0\n\ts_nop 1\n\ts_memrealtime %1\n\ts_nop 1\n\ts_memrealtime %2\n\ts_nop 1\n\ts_memrealtime %3\n\ts_nop 1\n\ts_memrealtime %4\n\ts_nop 1\n\t"
               "s_memrealtime %5\n\ts_nop 1\n\ts_memrealtime %6\n\ts_nop 1\n\ts_memrealtime %7\n\ts_nop 1\n\ts_memrealtime %8\n\ts_nop 1\n\ts_memrealtime %9\n\ts_waitcnt lgkmcnt(0)"
               : "=&s"(r0), "=&s"(r1), "=&s"(r2), "=&s"(r3), "=&s"(r4), "=&s"(r5), "=&s"(r6), "=&s"(r7), "=&s"(r8), "=&s"(r9), "=&s"(m0) :: "memory");
  const unsigned b = (unsigned)r0;
  unsigned acc = (b + 4u) * 24u - (unsigned)m0;
#define CO_STEP(r) asm volatile("s_cmp_eq_u32 %1, %2\n\ts_cselect_b32 vcc_lo, 12, 0\n\ts_sub_u32 %0, %0, vcc_lo" : "+s"(acc) : "s"((unsigned)r), "s"(b) : "scc", "vcc")
  CO_STEP(r1); CO_STEP(r2); CO_STEP(r3); CO_STEP(r4); CO_STEP(r5); CO_STEP(r6); CO_STEP(r7); CO_STEP(r8); CO_STEP(r9);
  return acc;
}
__device__ __forceinline__ float max3f(float a, float b, float c) { float r; asm("v_max3_f32 %0, %1, %2, %3" : "=v"(r) : "v"(a), "v"(b), "v"(c)); return r; }
__device__ __forceinline__ float max2f(float a, float b) { float r; asm("v_max_f32 %0, %1, %2" : "=v"(r) : "v"(a), "v"(b)); return r; }
__device__ __forceinline__ float xmax_g(float x) {
  float t;
  asm("v_mov_b32 %1, %0\n\ts_nop 1\n\tv_permlane32_swap_b32 %0, %1\n\tv_max_f32 %0, %0, %1\n\tv_mov_b32 %1, %0\n\ts_nop 1\n\tv_permlane16_swap_b32 %0, %1\n\tv_max_f32 %0, %0, %1"
      : "+v"(x), "=&v"(t));
  return x;
}
__device__ __forceinline__ float xsum_g(float x) {
  float t;
  asm("v_mov_b32 %1, %0\n\ts_nop 1\n\tv_permlane32_swap_b32 %0, %1\n\tv_add_f32 %0, %0, %1\n\tv_mov_b32 %1, %0\n\ts_nop 1\n\tv_permlane16_swap_b32 %0, %1\n\tv_add_f32 %0, %0, %1"
      : "+v"(x), "=&v"(t));
  return x;
}

struct Params {
  const u16* x; const u16* mem0; const u16* Wq; const u16* Wk; const u16* Wv; const u16* Wo;
  const u16* w_in; const u16* w_sp; const u16* b_sp; const u16* w_out;
  u16* out; char* ws;
};

__device__ __forceinline__ float gelu_tanh(float x) {
  const float u = 0.7978845608028654f * (x + 0.044715f * x * x * x);
  const float e = __builtin_amdgcn_exp2f(u * 2.885390081777927f);
  const float th = 1.f - 2.f * __builtin_amdgcn_rcpf(e + 1.f);
  return 0.5f * x * (1.f + th);
}

#define PSL 2
__device__ __forceinline__ void wait_flags(rsrc_t rF, unsigned off, unsigned want, int& budget) {
  const bool act = off != 0x80000000u;
  for (int spin = 0; spin < budget; ++spin) {
    const unsigned v = __builtin_amdgcn_raw_buffer_load_b32(rF, off, 0, 16);
    if (__builtin_amdgcn_ballot_w64(act && v < want) == 0) return;
    __builtin_amdgcn_s_sleep(PSL);
    asm volatile("" ::: "memory");
  }
  budget = 0;
}
__device__ __forceinline__ unsigned get_sel(rsrc_t rF, int& budget) {
  wait_flags(rF, (unsigned)(F_SEL * 128), 1, budget);
  return (__builtin_amdgcn_readfirstlane(__builtin_amdgcn_raw_buffer_load_b32(rF, (unsigned)(F_SEL * 128), 0, 16)) - 1u) & 1u;
}
__device__ __forceinline__ void put_flag(rsrc_t rF, unsigned off, unsigned v, int lane) {
  __builtin_amdgcn_raw_buffer_store_b32(v, rF, lane == 0 ? off : 0x80000000u, 0, 16);
}
template <int N> __device__ __forceinline__ void vmwait() { asm volatile("s_waitcnt vmcnt(%0)" :: "i"(N) : "memory"); }

template <int KB> __device__ __forceinline__ void warm(const void* base, unsigned total, unsigned off) {
  const unsigned o0 = off + (threadIdx.x >> 6) * KB * 1024 + (threadIdx.x & 63) * 16;
#pragma unroll 1
  for (int b = 0; b < KB; b += 16) {
    u32x4 t[16];
#pragma unroll
    for (int n = 0; n < 16; ++n) {
      unsigned o = o0 + (b + n) * 1024; o = (b + n < KB && o < total) ? o : 0u;
      asm volatile("global_load_dwordx4 %0, %1, %2" : "=v"(t[n]) : "v"(o), "s"(base) : "memory");
    }
    asm volatile("s_waitcnt vmcnt(0)" : "+v"(t[0]), "+v"(t[1]), "+v"(t[2]), "+v"(t[3]), "+v"(t[4]), "+v"(t[5]), "+v"(t[6]), "+v"(t[7]), "+v"(t[8]), "+v"(t[9]),
                 "+v"(t[10]), "+v"(t[11]), "+v"(t[12]), "+v"(t[13]), "+v"(t[14]), "+v"(t[15]) :: "memory");
  }
}

template <int NW> __device__ __forceinline__ void warm_cols(const u16* W, int xcd, int wg) {
  const unsigned wi = wg * 4 + (threadIdx.x >> 6);
  constexpr int PER = (1024 + NW - 1) / NW;
  u32x4 t[PER];
#pragma unroll
  for (int n = 0; n < PER; ++n) {
    unsigned i = wi + n * NW; i = i < 1024u ? i : 0u;
    const unsigned o = (i >> 1) * 8192u + xcd * 2048u + (i & 1) * 1024u + (threadIdx.x & 63) * 16u;
    asm volatile("global_load_dwordx4 %0, %1, %2" : "=v"(t[n]) : "v"(o), "s"(W) : "memory");
  }
  vmwait<0>();
#pragma unroll
  for (int n = 0; n < PER; ++n) asm volatile("" :: "v"(t[n]));
}

template <int KIND, int NT>
__device__ __forceinline__ void proj_role(const Params& p, LAS char* lds, int j, int s0, int s1, int fline, const char* wbase = nullptr, unsigned woff = 0) {
  constexpr int WRM = 0;
  const int tid = threadIdx.x, lane = tid & 63, wave = __builtin_amdgcn_readfirstlane(tid >> 6);
  const int g = lane >> 4, c = lane & 15;
  char* ws = p.ws;
  const unsigned lbase = (unsigned)(uintptr_t)lds;
  constexpr int NG = 4 * NT, RB = 128 * NT, LPR = 8 * NT, RPI = 8 / NT;
  const char* xn = ws + OFF_XN;
  bf16x8 w[NT][16];
  const int ldw = (KIND >= 2) ? 2 * FH : NH * DH;
  auto rotf = [](int row) { return NT == 1 ? ((row >> 1) & 1) + 2 * ((row >> 3) & 1) : (row & 3) + 4 * ((row >> 3) & 1); };
  constexpr int NBUF = NT == 4 ? 2 : 4, SI = 4 * NT;
  auto wissue = [&](int st) {
#pragma unroll 4
    for (int n = 0; n < SI; ++n) {
      const int row = (wave * SI + n) * RPI + lane / LPR;
      const int x = lane % LPR;
      const int G = ((x >> 1) - rotf(row)) & (NG - 1);
      const int k = st * 128 + row;
      const u16* src;
      if (KIND == 0) src = (G < 8 ? p.Wk : p.Wv) + (size_t)k * ldw + j * 128 + (G & 7) * 16 + (x & 1) * 8;
      else if (KIND == 1) src = p.Wq + (size_t)k * ldw + j * 256 + G * 16 + (x & 1) * 8;
      else if (KIND == 2) src = p.w_in + (size_t)k * ldw + j * 128 + G * 16 + (x & 1) * 8;
      else src = p.w_in + (size_t)k * ldw + FH + j * 64 + G * 16 + (x & 1) * 8;
      glds16(src, lbase + (st % NBUF) * 128 * RB + (wave * SI + n) * 1024);
    }
  };
#pragma unroll
  for (int st = 0; st < NBUF; ++st) wissue(st);
#pragma unroll
  for (int st = 0; st < 4; ++st) {
    if (NBUF == 2) { if (st < 3) vmwait<SI>(); else vmwait<0>(); }
    else { if (st == 0) vmwait<3 * SI>(); else if (st == 1) vmwait<2 * SI>(); else if (st == 2) vmwait<SI>(); else vmwait<0>(); }
    lds_barrier();
#pragma unroll
    for (int ks = 0; ks < 4; ++ks)
#pragma unroll
      for (int t = 0; t < NT; ++t) {
        bf16x4 pc[2];
#pragma unroll
        for (int u = 0; u < 2; ++u) {
          const int row = 32 * ks + 8 * g + 4 * u + (c >> 2);
          pc[u] = trrd(lds + (st % NBUF) * 128 * RB + row * RB + ((wave * NT + t + rotf(row)) & (NG - 1)) * 32 + (c & 3) * 8);
        }
        w[t][st * 4 + ks] = cat8(pc[0], pc[1]);
      }
    if (NBUF == 2 && st < 2) { lds_barrier(); wissue(st + 2); }
  }
  lds_barrier();
#pragma unroll
  for (int t = 0; t < NT; ++t)
#pragma unroll
    for (int k = 0; k < 16; ++k) asm volatile("" : "+a"(w[t][k]));
  const unsigned l16 = lane * 16;
  auto piece = [&](int s, int n) {
    const char* sb = xn + (size_t)s * 32768 + wave * 8192 + (n >> 2) * 4096;
    const unsigned m0v = lbase + (s % 3) * 32768 + wave * 8192 + (n >> 2) * 4096;
    if ((n & 3) == 0) glds_d<0>(l16, sb, m0v); else if ((n & 3) == 1) glds_d<1024>(l16, sb, m0v); else if ((n & 3) == 2) glds_d<2048>(l16, sb, m0v); else glds_d<3072>(l16, sb, m0v);
  };
  const rsrc_t rF = mkrsrc(ws + OFF_FLG, FLG_BYTES);
  int budget = SPIN_LIMIT;
  wait_flags(rF, (lane >= s0 && lane < s0 + 3) ? (unsigned)(F_XN * 128 + lane * 4) : 0x80000000u, 1, budget);
#pragma unroll
  for (int n = 0; n < 8; ++n) piece(s0, n);
#pragma unroll
  for (int n = 0; n < 8; ++n) piece(s0 + 1, n);
  const unsigned foff = fline * 128 + wave * 4;
  const unsigned fs = get_sel(rF, budget) ^ 1u;
  const rsrc_t rO = KIND == 0 ? mkrsrc(ws + OFF_CELL + (size_t)j * 4194304 + (wave >> 1) * 16384 + (wave & 1) * 4096, 4194304)
                  : KIND == 1 ? mkrsrc(ws + OFF_CELL + (size_t)(2 * j + (wave >> 1)) * 4194304 + 32768 + (wave & 1) * 4096, 4194304)
                  : KIND == 2 ? mkrsrc(ws + OFF_Z1 + (size_t)(j * 4 + wave) * 1024, NSEG * 57344) : mkrsrc(ws + OFF_Z2 + (size_t)(j * 4 + wave) * 1024, NSEG * 57344);
  auto run = [&](auto vf) {
    constexpr bool vform = decltype(vf)::value;
    auto emit = [&](f32x4 (&prv)[2][NT], int s, int ch) {
      if (KIND >= 2) {
#pragma unroll
        for (int q = 0; q < NT; ++q) { const int e = ch * NT + q; prv[e / (4 * NT)][(e / 4) % NT][e & 3] = gelu_tanh(prv[e / (4 * NT)][(e / 4) % NT][e & 3]); }
        if (KIND == 2 && (ch == 3 || ch == 7)) {
          const int T = ch >> 2; const f32x4 a0 = prv[T][0], a1 = prv[T][NT - 1];
          st128((u32x4){pk2(a0[0], a1[0]), pk2(a0[1], a1[1]), pk2(a0[2], a1[2]), pk2(a0[3], a1[3])}, rO, l16, s * 57344 + T * 28672);
        }
        if (KIND == 3 && ch == 7)
          st128((u32x4){pk2(prv[0][0][0], prv[0][0][1]), pk2(prv[0][0][2], prv[0][0][3]), pk2(prv[1][0][0], prv[1][0][1]), pk2(prv[1][0][2], prv[1][0][3])}, rO, l16, s * 57344);
      } else if (ch < 4) {
        const int t = ch % NT;
        u32x4 o;
        if (vform) o = (u32x4){pk2(prv[0][t][0], prv[0][t][1]), pk2(prv[0][t][2], prv[0][t][3]), pk2(prv[1][t][0], prv[1][t][1]), pk2(prv[1][t][2], prv[1][t][3])};
        else { const f32x4 a0 = prv[ch & 1][(ch & 2) % NT], a1 = prv[ch & 1][((ch & 2) + 1) % NT];
               o = (u32x4){pk2(a0[0], a1[0]), pk2(a0[1], a1[1]), pk2(a0[2], a1[2]), pk2(a0[3], a1[3])}; }
        st128(o, rO, l16, blk(s * 4, fs) + ch * 1024);
      }
    };
    auto mm = [&](f32x4& acc, const bf16x8& wv, const bf16x8& xv, bool first) {
      if (vform) { if (first) asm volatile("v_mfma_f32_16x16x32_bf16 %0, %1, %2, 0" : "=&v"(acc) : "v"(xv), "a"(wv) : "memory");
                   else asm volatile("v_mfma_f32_16x16x32_bf16 %0, %1, %2, %0" : "+v"(acc) : "v"(xv), "a"(wv) : "memory"); }
      else { if (first) asm volatile("v_mfma_f32_16x16x32_bf16 %0, %1, %2, 0" : "=&v"(acc) : "a"(wv), "v"(xv) : "memory");
             else asm volatile("v_mfma_f32_16x16x32_bf16 %0, %1, %2, %0" : "+v"(acc) : "a"(wv), "v"(xv) : "memory"); }
    };
    auto body = [&](auto em, auto pc, f32x4 (&acc)[2][NT], f32x4 (&prv)[2][NT], int s, int slot) {
      constexpr bool EM = decltype(em)::value, PC = decltype(pc)::value;
      LAS char* src = lds + slot * 32768 + lane * 16;
      const char* sb = xn + (size_t)(s + 2) * 32768 + wave * 8192;
      const unsigned m0v = lbase + (slot == 0 ? 2 : slot - 1) * 32768 + wave * 8192;
      bf16x8 xf[2][4];
#pragma unroll
      for (int q = 0; q < 4; ++q) xf[0][q] = *(LAS bf16x8*)(src + ((q & 1) * 16 + (q >> 1)) * 1024);
      if (EM) {
#pragma unroll
        for (int t = 0; t < NT; ++t) asm volatile("s_nop 3" : "+v"(prv[0][t]), "+v"(prv[1][t]) :: "memory");
      }
#pragma unroll
      for (int ch = 0; ch < 8; ++ch) {
#pragma unroll
        for (int i = 0; i < 4 * NT; ++i) {
          const int q = i / NT, t = i % NT;
          mm(acc[q & 1][t], w[t][2 * ch + (q >> 1)], xf[ch & 1][q], ch == 0 && q < 2);
          if (i < 4 && ch < 7) xf[(ch + 1) & 1][i] = *(LAS bf16x8*)(src + ((i & 1) * 16 + 2 * (ch + 1) + (i >> 1)) * 1024);
          if (EM && i == (NT == 4 ? 5 : NT == 2 ? 4 : 1)) emit(prv, s - 1, ch);
          if (PC && WRM && i == 3 && (ch == 1 || ch == 3 || ch == 5)) {
            unsigned o = woff + (wave * 37 + (s - s0) * 3 + (ch >> 1)) * 1024;
            o = ((s - s0) * 3 + (ch >> 1) < 37 && o < 1048576u) ? o : 0u;
            glds_s<0>(l16, wbase + o, lbase + 98304 + wave * 1024);
          }
          if (PC && i == (NT == 4 ? 10 : NT == 2 ? 6 : 2)) {
            if (ch == 0) glds_d<0>(l16, sb, m0v); else if (ch == 1) glds_d<1024>(l16, sb, m0v); else if (ch == 2) glds_d<2048>(l16, sb, m0v); else if (ch == 3) glds_d<3072>(l16, sb, m0v);
            else if (ch == 4) glds_d<0>(l16, sb + 4096, m0v + 4096); else if (ch == 5) glds_d<1024>(l16, sb + 4096, m0v + 4096);
            else if (ch == 6) glds_d<2048>(l16, sb + 4096, m0v + 4096); else glds_d<3072>(l16, sb + 4096, m0v + 4096);
          }
        }
      }
    };
    auto settle = [&](f32x4 (&acc)[2][NT]) {
#pragma unroll
      for (int t = 0; t < NT; ++t) asm volatile("s_nop 7" : "+v"(acc[0][t]), "+v"(acc[1][t]));
    };
    const std::true_type Y{}; const std::false_type N{};
    f32x4 accA[2][NT], accB[2][NT];
    int slot = s0 % 3;
    auto nxt = [&]() { slot = slot == 2 ? 0 : slot + 1; };
    vmwait<8>();
    for (int e = 0; e < 4; e += 2) {
      lds_barrier();
      body(N, Y, accA, accB, s0 + e, slot); nxt(); settle(accA);
#pragma unroll
      for (int ch = 0; ch < 8; ++ch) emit(accA, s0 + e, ch);
      vmwait<0>(); put_flag(rF, foff, s0 + e + 1, lane);
      if (e == 0) wait_flags(rF, (lane >= s0 && lane < s0 + 16) ? (unsigned)(F_XN * 128 + lane * 4) : 0x80000000u, 1, budget);
      lds_barrier();
      body(N, Y, accB, accA, s0 + e + 1, slot); nxt(); settle(accB);
#pragma unroll
      for (int ch = 0; ch < 8; ++ch) emit(accB, s0 + e + 1, ch);
      vmwait<0>(); put_flag(rF, foff, s0 + e + 2, lane);
    }
    lds_barrier();
    body(N, Y, accA, accB, s0 + 4, slot); nxt();
    vmwait<8>(); lds_barrier();
    put_flag(rF, foff, s0 + 4, lane);
    body(Y, Y, accB, accA, s0 + 5, slot); nxt();
    int s = s0 + 6;
    for (; s + 3 < s1; s += 2) {
      if (KIND < 2 && (s & 15) == 14) wait_flags(rF, (lane >= s + 2 && lane < s + 18) ? (unsigned)(F_XN * 128 + lane * 4) : 0x80000000u, 1, budget);
      if (s == s0 + 6) vmwait<0>(); else vmwait<9 + NT + WRM>();
      lds_barrier();
      put_flag(rF, foff, s == s0 + 6 ? s - 1 : s - 2, lane);
      body(Y, Y, accA, accB, s, slot); nxt();
      vmwait<9 + NT + WRM>(); lds_barrier();
      put_flag(rF, foff, s - 1, lane);
      body(Y, Y, accB, accA, s + 1, slot); nxt();
    }
    vmwait<9 + NT + WRM>(); lds_barrier();
    put_flag(rF, foff, s - 2, lane);
    body(Y, N, accA, accB, s, slot); nxt();
    vmwait<0>(); lds_barrier();
    put_flag(rF, foff, s, lane);
    body(Y, N, accB, accA, s + 1, slot); settle(accB);
#pragma unroll
    for (int ch = 0; ch < 8; ++ch) emit(accB, s + 1, ch);
  };
  if ((KIND == 0 && wave >= 2) || KIND == 3) run(std::true_type{}); else run(std::false_type{});
  vmwait<0>();
  put_flag(rF, foff, s1, lane);
  lds_barrier();
}

__device__ __forceinline__ float xmax_g(float x);
__device__ __forceinline__ float xsum_g(float x);
__device__ __forceinline__ void ah_role(const Params& p, LAS char* lds, int h, int sfirst, int fline) {
  auto nxt = [](int s) { return s < S_SW ? s + 1 : s + 2; };
  const int tid = threadIdx.x, lane = tid & 63, wave = __builtin_amdgcn_readfirstlane(tid >> 6);
  const int g = lane >> 4, c = lane & 15;
  char* ws = p.ws;
  const unsigned lbase = (unsigned)(uintptr_t)lds;
#pragma unroll 4
  for (int n = 0; n < 32; ++n) {
    const int e = wave * 32 + n;
    const int G = ((lane >> 2) - (e & 3)) & 15;
    glds16(p.Wo + (size_t)(h * DH + e) * DM + G * 32 + (lane & 3) * 8, lbase + e * 1024);
  }
  vmwait<0>();
  lds_barrier();
  bf16x8 wo[4][2][4];
#pragma unroll
  for (int P = 0; P < 4; ++P)
#pragma unroll
    for (int t = 0; t < 2; ++t)
#pragma unroll
      for (int m = 0; m < 4; ++m) {
        bf16x4 pc[2];
#pragma unroll
        for (int u = 0; u < 2; ++u) {
          const int e = 32 * m + 16 * u + 4 * g + (c >> 2);
          pc[u] = trrd(lds + e * 1024 + ((wave * 4 + P + (e & 3)) & 15) * 64 + (c & 3) * 16 + t * 8);
        }
        wo[P][t][m] = cat8(pc[0], pc[1]);
      }
  lds_barrier();
  const rsrc_t rF = mkrsrc(ws + OFF_FLG, FLG_BYTES);
  const rsrc_t rC = mkrsrc(ws + OFF_CELL + (size_t)h * 4194304, 4194304);
  const rsrc_t rKM = mkrsrc(ws + OFF_KM + (size_t)h * NSEG * 2048, NSEG * 2048);
  const rsrc_t rVM = mkrsrc(ws + OFF_VM + (size_t)h * NSEG * 2048, NSEG * 2048);
  const int T = wave & 1, eh = wave >> 1;
  const unsigned fAll = lane < 4 ? (unsigned)((F_KV + h) * 128 + lane * 4) : lane < 6 ? (unsigned)((F_Q + (h >> 1)) * 128 + ((h & 1) * 2 + lane - 4) * 4)
                      : lane < 8 ? (unsigned)((F_CH + 2 * h + lane - 6) * 128) : 0x80000000u;
  const int tq = 8 * (c >> 2) + 4 * T + (c & 3);
  const float CEXP = 0.08838834764831845f * 1.4426950408889634f;
  int budget = SPIN_LIMIT;
  const unsigned fs = get_sel(rF, budget) ^ 1u;
  u32x4 pf[6];
  auto issue = [&](int s) {
    const unsigned so = blk(s * 4, fs) + wave * 2048;
    pf[0] = ld128(rC, lane * 16, so, 16); pf[1] = ld128(rC, lane * 16, so + 1024, 16);
    pf[2] = ld128(rC, lane * 16, so + 32768, 16); pf[3] = ld128(rC, lane * 16, so + 32768 + 1024, 16);
    pf[4] = ld128(rC, lane * 16, so + 16384, 16); pf[5] = ld128(rC, lane * 16, so + 16384 + 1024, 16);
  };
  auto commit = [&](int par) {
    LAS char* d = lds + 16384 + par * 24576 + wave * 2048 + lane * 16;
#pragma unroll
    for (int n = 0; n < 6; ++n) *(LAS u32x4*)(d + (n >> 1) * 8192 + (n & 1) * 1024) = pf[n];
  };
  u32x4 km[4]; unsigned vm[4];
  auto memfetch = [&](int s) {
#pragma unroll
    for (int k4 = 0; k4 < 4; ++k4) km[k4] = ld128(rKM, (c & 1) * 256 + g * 64 + (c >> 2) * 16, s * 2048 + k4 * 512, 16);
#pragma unroll
    for (int n = 0; n < 4; ++n) vm[n] = __builtin_amdgcn_raw_buffer_load_b32(rVM, lane * 4, s * 2048 + (eh * 4 + n) * 256, 16);
  };
  wait_flags(rF, lane < 6 ? fAll : 0x80000000u, nxt(sfirst) + 1, budget);
  issue(sfirst); commit(0); issue(nxt(sfirst));
  lds_barrier();
  bool have = false;
  unsigned fl = 0;
  int par = 0, prev = 0;
  for (int s = sfirst; s < NSEG; prev = s + 1, s = nxt(s), par ^= 1) {
    const int s1 = nxt(s), s2 = nxt(s1);
    LAS char* in = lds + 16384 + par * 24576 + lane * 16;
    bf16x8 qx[4], kx[8], vx[4];
#pragma unroll
    for (int k4 = 0; k4 < 4; ++k4) {
      qx[k4] = *(LAS bf16x8*)(in + 8192 + (k4 * 2 + T) * 1024);
      kx[2 * k4] = *(LAS bf16x8*)(in + (k4 * 2) * 1024); kx[2 * k4 + 1] = *(LAS bf16x8*)(in + (k4 * 2 + 1) * 1024);
    }
#pragma unroll
    for (int n = 0; n < 4; ++n) vx[n] = *(LAS bf16x8*)(in + 16384 + (eh * 4 + n) * 1024);
    SB();
    f32x4 sx[2] = {}, sm = {};
#pragma unroll
    for (int k4 = 0; k4 < 4; ++k4) { sx[0] = mfma16(kx[2 * k4], qx[k4], sx[0]); sx[1] = mfma16(kx[2 * k4 + 1], qx[k4], sx[1]); }
    if (!have) {
      vmwait<0>();
      if (prev) put_flag(rF, fline * 128 + wave * 4, prev, lane);
      for (int spin = 0; spin < budget; ++spin) {
        const unsigned f2 = __builtin_amdgcn_raw_buffer_load_b32(rF, fAll, 0, 16);
        if (__builtin_amdgcn_ballot_w64(lane >= 6 && lane < 8 && f2 < (unsigned)(s + 1)) == 0) break;
        __builtin_amdgcn_s_sleep(PSL);
        asm volatile("" ::: "memory");
        if (spin == budget - 1) budget = 0;
      }
      memfetch(s);
      asm volatile("" : "+v"(km[0]), "+v"(km[1]), "+v"(km[2]), "+v"(km[3]), "+v"(vm[0]), "+v"(vm[1]), "+v"(vm[2]), "+v"(vm[3]));
    }
    float v[10];
#pragma unroll
    for (int T2 = 0; T2 < 2; ++T2)
#pragma unroll
      for (int r = 0; r < 4; ++r) v[T2 * 4 + r] = (8 * g + 4 * T2 + r <= tq) ? sx[T2][r] : -3.0e38f;
#pragma unroll
    for (int k4 = 0; k4 < 4; ++k4) sm = mfma16(asb8(km[k4]), qx[k4], sm);
    v[8] = sm[0]; v[9] = sm[1];
    float mm = v[0];
#pragma unroll
    for (int r = 1; r < 10; ++r) mm = fmaxf(mm, v[r]);
    mm = xmax_g(mm);
    float den = 0.f;
#pragma unroll
    for (int r = 0; r < 10; ++r) { v[r] = __builtin_amdgcn_exp2f((v[r] - mm) * CEXP); den += v[r]; }
    const float rinv = __builtin_amdgcn_rcpf(xsum_g(den));
    const bf16x8 pb = asb8((u32x4){pk2(v[0] * rinv, v[1] * rinv), pk2(v[2] * rinv, v[3] * rinv), pk2(v[4] * rinv, v[5] * rinv), pk2(v[6] * rinv, v[7] * rinv)});
    const bf16x8 pm = asb8((u32x4){pk2(v[8] * rinv, v[9] * rinv), 0, 0, 0});
    f32x4 o[4];
#pragma unroll
    for (int n = 0; n < 4; ++n) {
      o[n] = mfma16(vx[n], pb, (f32x4){});
      o[n] = mfma16(asb8((u32x4){vm[n], 0, 0, 0}), pm, o[n]);
    }
    LAS char* ob = lds + par * 8192;
#pragma unroll
    for (int m2 = 0; m2 < 2; ++m2)
      *(LAS u32x4*)(ob + (T * 4 + eh * 2 + m2) * 1024 + lane * 16) =
          (u32x4){pk2(o[2 * m2][0], o[2 * m2][1]), pk2(o[2 * m2][2], o[2 * m2][3]), pk2(o[2 * m2 + 1][0], o[2 * m2 + 1][1]), pk2(o[2 * m2 + 1][2], o[2 * m2 + 1][3])};
    const bool pub = have;
    asm volatile("s_waitcnt vmcnt(0)" : "+v"(fl) :: "memory");
    have = false;
    if (s1 < NSEG) commit(par ^ 1);
    asm volatile("" ::: "memory");
    if (pub) put_flag(rF, fline * 128 + wave * 4, prev, lane);
    if (s1 < NSEG) {
      have = __builtin_amdgcn_ballot_w64(lane >= 6 && lane < 8 && fl < (unsigned)(s1 + 1)) == 0;
      if (have) memfetch(s1);
      if (s2 < NSEG) {
        if (__builtin_amdgcn_ballot_w64(lane < 6 && fl < (unsigned)(s2 + 1)) != 0) wait_flags(rF, lane < 6 ? fAll : 0x80000000u, s2 + 1, budget);
        issue(s2);
      }
      fl = __builtin_amdgcn_raw_buffer_load_b32(rF, fAll, 0, 16);
    }
    lds_barrier();
    bf16x8 of[8];
#pragma unroll
    for (int n = 0; n < 8; ++n) of[n] = *(LAS bf16x8*)(ob + n * 1024 + lane * 16);
    f32x4 a0[2], a1[2];
#pragma unroll
    for (int i = 0; i <= 8; ++i) {
      if (i < 8) {
        const int T2 = i >> 2, P = i & 3;
        MF0(a0[i & 1], wo[P][0][0], of[T2 * 4]); MF0(a1[i & 1], wo[P][1][0], of[T2 * 4]);
        MF(a0[i & 1], wo[P][0][1], of[T2 * 4 + 1]); MF(a1[i & 1], wo[P][1][1], of[T2 * 4 + 1]);
      }
      if (i > 0) {
        const int j = i - 1;
        if (i == 8) asm volatile("s_nop 7" : "+v"(a0[j & 1]), "+v"(a1[j & 1])); else asm volatile("" : "+v"(a0[j & 1]), "+v"(a1[j & 1]));
        const f32x4 b0 = a0[j & 1], b1 = a1[j & 1];
        st128((u32x4){pk2(b0[0], b0[1]), pk2(b0[2], b0[3]), pk2(b1[0], b1[1]), pk2(b1[2], b1[3])}, rC, lane * 16,
              blk(s * 4, fs) + ((j >> 2) * 2 + (wave >> 1)) * 16384 + ((wave & 1) * 4 + (j & 3)) * 1024);
      }
      if (i < 8) {
        const int T2 = i >> 2, P = i & 3;
        MF(a0[i & 1], wo[P][0][2], of[T2 * 4 + 2]); MF(a1[i & 1], wo[P][1][2], of[T2 * 4 + 2]);
        MF(a0[i & 1], wo[P][0][3], of[T2 * 4 + 3]); MF(a1[i & 1], wo[P][1][3], of[T2 * 4 + 3]);
      }
    }
  }
  vmwait<0>();
  put_flag(rF, fline * 128 + wave * 4, NSEG, lane);
}

__device__ __forceinline__ void red_role(const Params& p, LAS char* lds, int r) {
  const int tid = threadIdx.x, lane = tid & 63, wave = __builtin_amdgcn_readfirstlane(tid >> 6);
  const int g = lane >> 4, c = lane & 15;
  char* ws = p.ws;
  const rsrc_t rF = mkrsrc(ws + OFF_FLG, FLG_BYTES);
  const rsrc_t rS = mkrsrc(ws + OFF_CELL + (size_t)wave * 8 * 4194304, 8 * 4194304);
  const rsrc_t rY = mkrsrc(ws + OFF_Y, 7 * 4194304);
  const int T = r >> 4, w = (r >> 2) & 3, P = r & 3;
  bf16x8 a0, a1;
#pragma unroll
  for (int j = 0; j < 8; ++j) { a0[j] = (__bf16)((8 * g + j == c) ? 1.f : 0.f); a1[j] = (__bf16)((8 * g + j == 16 + c) ? 1.f : 0.f); }
  const unsigned fA = lane < 16 ? (unsigned)(((lane < 8 ? F_AH : F_AH2) + wave * 8 + (lane & 7)) * 128 + w * 4) : 0x80000000u;
  auto mine = [&](int s) { return (lane >> 3) == ((s > S_SW && (s & 1)) ? 1 : 0); };
  const int tok = 8 * (c >> 2) + 4 * T + (c & 3);
  int budget = SPIN_LIMIT;
  const unsigned fs = get_sel(rF, budget) ^ 1u;
  unsigned fl = 0;
  u32x4 v[10]; u32x2 xv = {0, 0};
  auto fetch = [&](int s) {
    if ((s & 7) == 0) {
      const unsigned fY = (lane < 8 && wave + 4 * (lane >> 2) < 7) ? (unsigned)((F_SP + (s >> 3) * 7 + wave + 4 * (lane >> 2)) * 128 + (lane & 3) * 4) : 0x80000000u;
      wait_flags(rF, fY, 1, budget);
    }
    const unsigned sb = blk(s * 4, fs) + (r >> 3) * 16384 + (r & 7) * 1024;
    v[8] = ld128(rY, lane * 16, sb + wave * 4194304, 16);
    v[9] = ld128(rY, wave < 3 ? (unsigned)(lane * 16) : 0x80000000u, sb + (4 + (wave < 3 ? wave : 0)) * 4194304, 16);
    if (wave < 2) xv = *(const u32x2*)(p.x + (size_t)(s * 32 + tok) * DM + 128 * w + 32 * P + 16 * wave + 4 * g);
    for (int spin = 0; __builtin_amdgcn_ballot_w64(mine(s) && fl < (unsigned)(s + 1)) != 0 && spin < budget; ++spin) {
      if (spin) __builtin_amdgcn_s_sleep(PSL);
      asm volatile("" ::: "memory");
      fl = __builtin_amdgcn_raw_buffer_load_b32(rF, fA, 0, 16);
      if (spin == budget - 1) budget = 0;
    }
#pragma unroll
    for (int n = 0; n < 8; ++n) v[n] = ld128(rS, lane * 16, sb + n * 4194304, 16);
  };
  fetch(0);
  for (int s = 0; s < NSEG; ++s) {
    f32x4 d0 = {}, d1 = {};
#pragma unroll
    for (int n = 9; n >= 0; --n) { const int q = n >= 8 ? n : 7 - n; d0 = mfma16(a0, asb8(v[q]), d0); d1 = mfma16(a1, asb8(v[q]), d1); }
    const u32x2 xc = xv;
    LAS char* b = lds + (s & 1) * 8192;
    *(LAS f32x4*)(b + (wave * 2) * 1024 + lane * 16) = d0;
    *(LAS f32x4*)(b + (wave * 2 + 1) * 1024 + lane * 16) = d1;
    const bool ahead = ((s + 1) & 7) != 0 && __builtin_amdgcn_ballot_w64(mine(s + 1) && fl < (unsigned)(s + 2)) == 0;
    if (ahead) fetch(s + 1);
    lds_barrier();
    if (wave < 2) {
      f32x4 t = *(LAS f32x4*)(b + wave * 1024 + lane * 16);
#pragma unroll
      for (int n = 1; n < 4; ++n) t += *(LAS f32x4*)(b + (n * 2 + wave) * 1024 + lane * 16);
      *(u32x2*)(p.out + (size_t)(s * 32 + tok) * DM + 128 * w + 32 * P + 16 * wave + 4 * g) =
          (u32x2){pk2(t[0] + bflo(xc.x), t[1] + bfhi(xc.x)), pk2(t[2] + bflo(xc.y), t[3] + bfhi(xc.y))};
    }
    if (!ahead && s + 1 < NSEG) fetch(s + 1);
  }
}

__device__ __forceinline__ float rowsum16(float x) {
  x += __builtin_bit_cast(float, __builtin_amdgcn_update_dpp(0, __builtin_bit_cast(int, x), 0x128, 0xf, 0xf, false));
  x += __builtin_bit_cast(float, __builtin_amdgcn_update_dpp(0, __builtin_bit_cast(int, x), 0x124, 0xf, 0xf, false));
  x += __builtin_bit_cast(float, __builtin_amdgcn_update_dpp(0, __builtin_bit_cast(int, x), 0x122, 0xf, 0xf, false));
  x += __builtin_bit_cast(float, __builtin_amdgcn_update_dpp(0, __builtin_bit_cast(int, x), 0x121, 0xf, 0xf, false));
  return x;
}
__device__ __forceinline__ void sp_role(const Params& p, LAS char* lds, int tile) {
  const int tid = threadIdx.x, lane = tid & 63, wave = __builtin_amdgcn_readfirstlane(tid >> 6);
  const int g = lane >> 4, c = lane & 15;
  char* ws = p.ws;
  const unsigned lbase = (unsigned)(uintptr_t)lds;
  const rsrc_t rF = mkrsrc(ws + OFF_FLG, FLG_BYTES);
  int budget = SPIN_LIMIT;
  proj_role<3, 1>(p, lds, tile % 14, (tile / 14) * 16, (tile / 14) * 16 + 16, F_Z2 + tile, (const char*)p.w_sp + (size_t)(tile / 7) * 1048576, (tile % 7) * 151552);
  for (int s = tile; s < NSEG; s += 56) {
    const rsrc_t rZ = mkrsrc(ws + OFF_Z2 + (size_t)s * 57344, 57344);
    const rsrc_t rN = mkrsrc(ws + OFF_ZN + (size_t)s * 57344, 57344);
    wait_flags(rF, lane < 14 ? (unsigned)((F_Z2 + (s >> 4) * 14 + ((wave * 14 + lane) >> 2)) * 128 + ((wave * 14 + lane) & 3) * 4) : 0x80000000u, s + 1, budget);
    u32x4 z[14];
#pragma unroll
    for (int n = 0; n < 14; ++n) z[n] = ld128(rZ, lane * 16, (wave * 14 + n) * 1024, 16);
    float f[14][8], sum[8] = {};
#pragma unroll
    for (int n = 0; n < 14; ++n) {
      f[n][0] = bflo(z[n].x); f[n][1] = bfhi(z[n].x); f[n][2] = bflo(z[n].y); f[n][3] = bfhi(z[n].y);
      f[n][4] = bflo(z[n].z); f[n][5] = bfhi(z[n].z); f[n][6] = bflo(z[n].w); f[n][7] = bfhi(z[n].w);
#pragma unroll
      for (int j = 0; j < 8; ++j) sum[j] += f[n][j];
    }
    LAS float* red = (LAS float*)lds;
#pragma unroll
    for (int j = 0; j < 8; ++j) { const float t = rowsum16(sum[j]); if (c == 0) red[wave * 32 + g * 8 + j] = t; }
    lds_barrier();
    float q[8] = {};
#pragma unroll
    for (int j = 0; j < 8; ++j) {
      const float mean = (red[g * 8 + j] + red[32 + g * 8 + j] + red[64 + g * 8 + j] + red[96 + g * 8 + j]) * (1.f / FH);
#pragma unroll
      for (int n = 0; n < 14; ++n) { f[n][j] -= mean; q[j] += f[n][j] * f[n][j]; }
    }
#pragma unroll
    for (int j = 0; j < 8; ++j) { const float t = rowsum16(q[j]); if (c == 0) red[128 + wave * 32 + g * 8 + j] = t; }
    lds_barrier();
    float rs[8];
#pragma unroll
    for (int j = 0; j < 8; ++j)
      rs[j] = 1.f / sqrtf((red[128 + g * 8 + j] + red[160 + g * 8 + j] + red[192 + g * 8 + j] + red[224 + g * 8 + j]) * (1.f / FH) + 1e-5f);
#pragma unroll
    for (int n = 0; n < 14; ++n)
      st128((u32x4){pk2(f[n][0] * rs[0], f[n][1] * rs[1]), pk2(f[n][2] * rs[2], f[n][3] * rs[3]), pk2(f[n][4] * rs[4], f[n][5] * rs[5]), pk2(f[n][6] * rs[6], f[n][7] * rs[7])},
            rN, lane * 16, (wave * 14 + n) * 1024);
    vmwait<0>();
    lds_barrier();
    put_flag(rF, (F_LN + s) * 128, 1, tid);
  }
  const int tp = tile / 7, fc = tile % 7;
  if (wave == 0) {
    wait_flags(rF, (unsigned)((F_LN + lane) * 128), 1, budget);
    __builtin_amdgcn_fence(__ATOMIC_ACQUIRE, "agent");
    vmwait<0>();
  }
  lds_barrier();
  const char* zn = ws + OFF_ZN;
  unsigned boff[4];
#pragma unroll
  for (int Tt = 0; Tt < 4; ++Tt) boff[Tt] = (unsigned)(((Tt >> 1) * 32 + 8 * (c >> 2) + 4 * (Tt & 1) + (c & 3)) * S_ + 8 * g) * 2;
  const char* wsp = (const char*)(p.w_sp + (size_t)(tp * 256 + wave * 64) * S_);
  const unsigned l16 = lane * 16;
  auto piece = [&](int kk, int n) {
    const unsigned dst = lbase + (kk % 3) * 49152;
    if (n < 4) {
      const char* sb = zn + ((size_t)(kk * 2 + (wave >> 1)) * 56 + fc * 8 + (wave & 1) * 4) * 1024;
      const unsigned m0v = dst + wave * 4096;
      if (n == 0) glds_s<0>(l16, sb, m0v); else if (n == 1) glds_s<1024>(l16, sb, m0v); else if (n == 2) glds_s<2048>(l16, sb, m0v); else glds_s<3072>(l16, sb, m0v);
    } else {
      const int q = n - 4;
      glds_s<0>(boff[q >> 1], wsp + kk * 128 + (q & 1) * 64, dst + 16384 + (wave * 8 + (q & 1) * 4 + (q >> 1)) * 1024);
    }
  };
  f32x4 acc[4][8] = {};
#pragma unroll
  for (int n = 0; n < 12; ++n) piece(0, n);
#pragma unroll
  for (int n = 0; n < 12; ++n) piece(1, n);
  for (int kk = 0; kk < 32; ++kk) {
    if (kk + 1 < 32) vmwait<12>(); else vmwait<0>();
    lds_barrier();
    LAS char* b = lds + (kk % 3) * 49152 + lane * 16;
    bf16x8 bf[2][4], af[2][4];
#pragma unroll
    for (int Tt = 0; Tt < 4; ++Tt) bf[0][Tt] = *(LAS bf16x8*)(b + 16384 + (wave * 8 + Tt) * 1024);
#pragma unroll
    for (int q = 0; q < 4; ++q) af[0][q] = *(LAS bf16x8*)(b + q * 1024);
#pragma unroll
    for (int ch = 0; ch < 4; ++ch) {
      SB();
      if (ch < 3) {
#pragma unroll
        for (int q = 0; q < 4; ++q) af[(ch + 1) & 1][q] = *(LAS bf16x8*)(b + ((ch + 1) * 4 + q) * 1024);
      }
      if (ch == 0) {
#pragma unroll
        for (int Tt = 0; Tt < 4; ++Tt) bf[1][Tt] = *(LAS bf16x8*)(b + 16384 + (wave * 8 + 4 + Tt) * 1024);
      }
#pragma unroll
      for (int q = 0; q < 4; ++q) {
#pragma unroll
        for (int Tt = 0; Tt < 4; ++Tt) acc[Tt][(ch & 1) * 4 + q] = mfma16(af[ch & 1][q], bf[ch >> 1][Tt], acc[Tt][(ch & 1) * 4 + q]);
        if (q < 3) { SB(); if (kk + 2 < 32) piece(kk + 2, ch * 3 + q); SB(); }
      }
    }
    SB();
  }
  lds_barrier();
#pragma unroll 4
  for (int n = 0; n < 32; ++n) {
    const int e = wave * 32 + n;
    const int G = ((lane >> 2) - (e & 3)) & 15;
    glds16(p.w_out + (size_t)(fc * 128 + e) * DM + G * 32 + (lane & 3) * 8, lbase + e * 1024);
  }
  {
    const int rg = tp / 2;
    wait_flags(rF, lane < 4 ? (unsigned)((F_Z1 + rg * 7 + fc) * 128 + lane * 4) : 0x80000000u, tp * 8 + 8, budget);
  }
  const rsrc_t rZ1 = mkrsrc(ws + OFF_Z1, NSEG * 2 * 28 * 1024);
  bf16x8 ub[4][4];
#pragma unroll
  for (int Tt = 0; Tt < 4; ++Tt) {
    const int sg = tp * 8 + wave * 2 + (Tt >> 1);
    const float bias = bflo((unsigned)p.b_sp[sg * 32 + 8 * (c >> 2) + 4 * (Tt & 1) + (c & 3)]);
#pragma unroll
    for (int m = 0; m < 4; ++m) {
      const u32x4 z = ld128(rZ1, lane * 16, ((sg * 2 + (Tt & 1)) * 28 + fc * 4 + m) * 1024, 16);
      const f32x4 g0 = acc[Tt][2 * m] + bias, g1 = acc[Tt][2 * m + 1] + bias;
      ub[Tt][m] = asb8((u32x4){pk2(bflo(z.x) * g0[0], bflo(z.y) * g0[1]), pk2(bflo(z.z) * g0[2], bflo(z.w) * g0[3]),
                               pk2(bfhi(z.x) * g1[0], bfhi(z.y) * g1[1]), pk2(bfhi(z.z) * g1[2], bfhi(z.w) * g1[3])});
    }
  }
  vmwait<0>();
  lds_barrier();
  const rsrc_t rS = mkrsrc(ws + OFF_Y + (size_t)fc * 4194304, 4194304);
  const unsigned fs = get_sel(rF, budget) ^ 1u;
  for (int P16 = 0; P16 < 16; ++P16) {
    bf16x8 wf[2][4];
#pragma unroll
    for (int t = 0; t < 2; ++t)
#pragma unroll
      for (int m = 0; m < 4; ++m) {
        bf16x4 pc[2];
#pragma unroll
        for (int u = 0; u < 2; ++u) {
          const int e = 32 * m + 16 * u + 4 * g + (c >> 2);
          pc[u] = trrd(lds + e * 1024 + ((P16 + (e & 3)) & 15) * 64 + (c & 3) * 16 + t * 8);
        }
        wf[t][m] = cat8(pc[0], pc[1]);
      }
#pragma unroll
    for (int Tt = 0; Tt < 4; ++Tt) {
      f32x4 a0 = {}, a1 = {};
#pragma unroll
      for (int m = 0; m < 4; ++m) { a0 = mfma16(wf[0][m], ub[Tt][m], a0); a1 = mfma16(wf[1][m], ub[Tt][m], a1); }
      const int sg = tp * 8 + wave * 2 + (Tt >> 1);
      st128((u32x4){pk2(a0[0], a0[1]), pk2(a0[2], a0[3]), pk2(a1[0], a1[1]), pk2(a1[2], a1[3])}, rS, lane * 16,
            blk(sg * 4, fs) + ((Tt & 1) * 2 + (P16 >> 3)) * 16384 + (P16 & 7) * 1024);
    }
  }
  vmwait<0>();
  put_flag(rF, (F_SP + tile) * 128 + wave * 4, 1, lane);
}

__device__ __forceinline__ float wavesum(float x) {
  x = rowsum16(x);
  return xsum_g(x);
}
__device__ __forceinline__ void ln_role(const Params& p, LAS char* lds, int i) {
  const int tid = threadIdx.x, lane = tid & 63, wave = __builtin_amdgcn_readfirstlane(tid >> 6);
  char* ws = p.ws;
  u32x4 v[8];
#pragma unroll
  for (int r = 0; r < 8; ++r) v[r] = *(const u32x4*)(p.x + (size_t)(i * 32 + wave * 8 + r) * DM + lane * 8);
  float f[8][8], m[8];
#pragma unroll
  for (int r = 0; r < 8; ++r) {
    f[r][0] = bflo(v[r].x); f[r][1] = bfhi(v[r].x); f[r][2] = bflo(v[r].y); f[r][3] = bfhi(v[r].y);
    f[r][4] = bflo(v[r].z); f[r][5] = bfhi(v[r].z); f[r][6] = bflo(v[r].w); f[r][7] = bfhi(v[r].w);
    m[r] = rowsum16(((f[r][0] + f[r][1]) + (f[r][2] + f[r][3])) + ((f[r][4] + f[r][5]) + (f[r][6] + f[r][7])));
  }
#pragma unroll
  for (int r = 0; r < 8; ++r) {
    const float mean = xsum_g(m[r]) * (1.f / DM);
    float q = 0.f;
#pragma unroll
    for (int j = 0; j < 8; ++j) { f[r][j] -= mean; q += f[r][j] * f[r][j]; }
    m[r] = rowsum16(q);
  }
#pragma unroll
  for (int r = 0; r < 8; ++r) {
    const float rs = 1.f / sqrtf(xsum_g(m[r]) * (1.f / DM) + 1e-5f);
    const int T = (r >> 2) & 1, ii = 4 * wave + (r & 3), ks = lane >> 2;
    *(LAS u32x4*)(lds + (T * 16 + ks) * 1024 + (((lane & 3) * 16 + ii + ks) & 63) * 16) =
        (u32x4){pk2(f[r][0] * rs, f[r][1] * rs), pk2(f[r][2] * rs, f[r][3] * rs), pk2(f[r][4] * rs, f[r][5] * rs), pk2(f[r][6] * rs, f[r][7] * rs)};
  }
  lds_barrier();
  const rsrc_t rN = mkrsrc(ws + OFF_XN + (size_t)i * 32768, 32768);
#pragma unroll
  for (int n = 0; n < 8; ++n) {
    const int fr = wave * 8 + n;
    st128(*(LAS u32x4*)(lds + fr * 1024 + ((lane + (fr & 15)) & 63) * 16), rN, lane * 16, fr * 1024);
  }
  vmwait<0>();
  lds_barrier();
  put_flag(mkrsrc(ws + OFF_FLG, FLG_BYTES), (F_XN + (i >> 5)) * 128 + (i & 31) * 4, 1, tid);
}
__device__ __forceinline__ void init_role(const Params& p) {
  const int tid = threadIdx.x;
  char* ws = p.ws;
  const rsrc_t rX = mkrsrc(ws + OFF_X, X_ZERO);
  if (tid >= 64) {
    for (int q = tid - 64; q < 512; q += 192) {
      u32x4 o;
#pragma unroll
      for (int j = 0; j < 4; ++j) {
        const int d = q * 4 + j;
        const int line = d >> 5, rp = (d >> 4) & 1, cc = (d >> 1) & 7, t0 = (d & 1) * 2;
        const int bb = line & 1, g = (line >> 1) & 3, sk = line >> 3;
        const int r = 2 * g + rp, k0 = 64 * sk + 16 * t0 + 8 * bb + cc;
        const unsigned v = (unsigned)p.mem0[r * DM + k0] | ((unsigned)p.mem0[r * DM + k0 + 16] << 16);
        o[j] = v > 1u ? v : 1u;
      }
      st128(o, rX, q * 16, X_M); st128(o, rX, q * 16, X_M + 8192);
    }
  } else if (tid == 0) {
    const char* base = ws + OFF_X + X_ZERO + 16384;
    unsigned best[2] = {~0u, ~0u};
    for (int r = 0; r < 5; ++r)
      for (int par = 0; par < 2; ++par) {
        const char* a = base + par * 8192 + 1024;
        const unsigned t0 = clk32();
        asm volatile("global_store_dword %0, %1, off sc1\n\ts_waitcnt vmcnt(0)" :: "v"(a), "v"(t0) : "memory");
        const unsigned t1 = clk32();
        best[par] = min(best[par], t1 - t0);
      }
    const unsigned xcc = (unsigned)__builtin_amdgcn_s_getreg(20 | (3 << 11)) & 0xfu;
    const unsigned sel = ((best[1] < best[0]) ? 1u : 0u) ^ ((xcc >> 2) & 1u);
    __builtin_amdgcn_raw_buffer_store_b128((u32x4){sel + 1u, best[0], best[1], xcc}, rX, OFF_FLG + F_SEL * 128, 0, 16);
  }
}

constexpr int L_MST = 0, L_KX = 8192, L_VX = 16384, L_QI = 24576, L_OI = 30976, L_KVX = 2048, L_WO = 32768, L_S1 = 98304, L_WV = 98304, L_KV1 = 147456;
constexpr int QI_G = 400, QI_W = 4 * QI_G;
#define CA 2650
#define CB 3650

__device__ __forceinline__ unsigned min3u(unsigned a, unsigned b, unsigned c) { return min(min(a, b), c); }
__device__ __forceinline__ unsigned min4u(u32x4 a) { return min(min3u(a.x, a.y, a.z), a.w); }

__device__ __forceinline__ void chain_role(const Params& p, LAS char* lds, int h, int a) {
  const int tid = threadIdx.x, lane = tid & 63, wave = __builtin_amdgcn_readfirstlane(tid >> 6);
  const int g = lane >> 4, c = lane & 15;
  char* ws = p.ws;
  const unsigned lbase = (unsigned)(uintptr_t)lds;

  const rsrc_t rX = mkrsrc(ws + OFF_X, X_ZERO);
  int spin_limit = SPIN_LIMIT;
  unsigned sel = 0;
  bf16x16 wq[2][8], wk[2][8], wv[2][8];
  {
    auto issue = [&](const u16* W, int half, int soff) {
#pragma unroll 4
      for (int n = 0; n < 16; ++n) {
        const int row = (wave * 16 + n) * 4 + (lane >> 4);
        const int x = lane & 15;
        const int rot = (row & 3) + 4 * ((row >> 4) & 1);
        const int G = ((x >> 1) - rot) & 7;
        glds16(W + (size_t)(half * 256 + row) * (NH * DH) + h * DH + G * 16 + (x & 1) * 8, lbase + soff + (wave * 16 + n) * 1024);
      }
    };
    auto fetch = [&](bf16x16 (&w)[2][8], int half, int soff) {
#pragma unroll
      for (int m = 0; m < 4; ++m) {
        const int s4 = (wave + m) & 3;
#pragma unroll
        for (int t = 0; t < 2; ++t) {
          bf16x4 pc[4];
#pragma unroll
          for (int u = 0; u < 4; ++u) {
            const int q = c >> 2;
            const int row = 64 * s4 + 32 * (u & 1) + 16 * (q & 1) + 8 * (u >> 1) + 4 * (g >> 1) + 2 * (q >> 1) + (g & 1);
            const int rot = (row & 3) + 4 * ((row >> 4) & 1);
            pc[u] = trrd(lds + soff + row * 256 + ((wave * 2 + t + rot) & 7) * 32 + (c & 3) * 8);
          }
          w[t][2 * m + half] = cat16(cat8(pc[0], pc[1]), cat8(pc[2], pc[3]));
        }
      }
    };
    issue(p.Wq, 0, 0); issue(p.Wq, 1, L_S1);
    asm volatile("s_waitcnt vmcnt(16)" ::: "memory"); lds_barrier();
    fetch(wq, 0, 0); lds_barrier();
    issue(p.Wk, 0, 0);
    asm volatile("s_waitcnt vmcnt(16)" ::: "memory"); lds_barrier();
    fetch(wq, 1, L_S1); lds_barrier();
    unsigned vsel;
    asm volatile("buffer_load_dword %0, %1, %2, 0 offen sc1" : "=&v"(vsel) : "v"((unsigned)(OFF_FLG + F_SEL * 128)), "s"(rX) : "memory");
    issue(p.Wk, 1, L_S1);
    asm volatile("s_waitcnt vmcnt(16)" : "+v"(vsel) :: "memory"); lds_barrier();
    sel = __builtin_amdgcn_readfirstlane(vsel);
    if (__builtin_expect(sel == 0, 0)) {
      wait_flags(rX, (unsigned)(OFF_FLG + F_SEL * 128), 1, spin_limit);
      sel = __builtin_amdgcn_readfirstlane(__builtin_amdgcn_raw_buffer_load_b32(rX, (unsigned)(OFF_FLG + F_SEL * 128), 0, 16));
    }
    sel = (sel - 1u) & 1u;
    fetch(wk, 0, 0); lds_barrier();
    issue(p.Wv, 0, 0);
    asm volatile("s_waitcnt vmcnt(16)" ::: "memory"); lds_barrier();
    fetch(wk, 1, L_S1); lds_barrier();
    unsigned x0, x1, x2, x3, x4, x5;
    {
      const int ml = ((a * 4 + wave) * 4 + g) * 2 + (c >> 3), rl = h * 2 + a;
      const unsigned o0 = (ml >> 1) * 16384 + sel * 8192 + (ml & 1) * 4096, o1 = (rl >> 1) * 16384 + sel * 8192 + (rl & 1) * 4096;
      const char* t0 = ws + OFF_CELL + (size_t)h * 4194304 + blk(lane * 4 + (wave >> 1), sel ^ 1u) + (wave & 1) * 4096 + 2048 + a * 64;
      const char* t1 = ws + (a ? OFF_VM : OFF_KM) + (size_t)h * NSEG * 2048 + (lane & 31) * 4096;
      asm volatile("buffer_load_dword %0, %6, %8, 0 offen sc1\n\tbuffer_load_dword %1, %6, %8, %9 offen sc1\n\t"
                   "buffer_load_dword %2, %7, %8, 0 offen sc1\n\tbuffer_load_dword %3, %7, %8, %9 offen sc1\n\t"
                   "global_load_dword %4, %10, off sc1\n\tglobal_load_dword %5, %11, off sc1"
                   : "=&v"(x0), "=&v"(x1), "=&v"(x2), "=&v"(x3), "=&v"(x4), "=&v"(x5) : "v"(o0), "v"(o1), "s"(rX), "s"(524288u), "v"(t0), "v"(t1) : "memory");
    }
    issue(p.Wv, 1, L_S1);
    asm volatile("s_waitcnt vmcnt(16)" :: "v"(x0), "v"(x1), "v"(x2), "v"(x3), "v"(x4), "v"(x5) : "memory"); lds_barrier();
    fetch(wv, 0, 0); lds_barrier();
#pragma unroll 4
    for (int n = 0; n < 16; ++n) {
      const int e = (wave * 16 + n) * 2 + (lane >> 5);
      const int x = lane & 31;
      const int rot = (e & 1) + 2 * ((e >> 2) & 1) + 4 * ((e >> 3) & 1);
      const int G = ((x >> 1) - rot) & 15;
      glds16(p.Wo + (size_t)(h * DH + e) * DM + a * 256 + G * 16 + (x & 1) * 8, lbase + L_WO + (wave * 16 + n) * 1024);
    }
    asm volatile("s_waitcnt vmcnt(16)" ::: "memory"); lds_barrier();
    fetch(wv, 1, L_S1);
    asm volatile("s_waitcnt vmcnt(0)" ::: "memory"); lds_barrier();
    {
      bf16x8 wlo[8], whi[8];
#pragma unroll
      for (int n = 0; n < 8; ++n) {
        const int q = c >> 2, rot = (q & 1) + 2 * (g & 1) + 4 * (q >> 1);
        LAS char* b = lds + L_WO + (64 * (n >> 2) + 16 * (g >> 1) + 8 * (q >> 1) + 4 * (g & 1) + (q & 1)) * 512 + ((wave * 4 + (n & 3) + rot) & 15) * 32 + (c & 3) * 8;
        wlo[n] = cat8(trrd(b), trrd(b + 2 * 512)); whi[n] = cat8(trrd(b + 32 * 512), trrd(b + 34 * 512));
      }
      lds_barrier();
#pragma unroll
      for (int n = 0; n < 8; ++n) { LAS char* d = lds + L_WO + (wave * 8 + n) * 2048 + lane * 16; *(LAS bf16x8*)d = wlo[n]; *(LAS bf16x8*)(d + 1024) = whi[n]; }
    }
#pragma unroll
    for (int t = 0; t < 2; ++t)
#pragma unroll
      for (int u = 5; u < 8; ++u) {
        LAS char* d = lds + L_WV + (wave * 6 + (u == 5 ? 4 : (u - 6) * 2) + t) * 2048 + lane * 16;
        *(LAS bf16x8*)d = __builtin_shufflevector(wv[t][u], wv[t][u], 0, 1, 2, 3, 4, 5, 6, 7);
        *(LAS bf16x8*)(d + 1024) = __builtin_shufflevector(wv[t][u], wv[t][u], 8, 9, 10, 11, 12, 13, 14, 15);
      }
#pragma unroll
    for (int t = 0; t < 2; ++t)
#pragma unroll
      for (int u = 0; u < 8; ++u) {
        asm volatile("" : "+a"(wk[t][u]));
        if (u < 5) asm volatile("" : "+a"(wv[t][u])); else asm volatile("" : "+a"(wq[t][u]));
      }
  }

  const rsrc_t rKVM = mkrsrc(ws + (a ? OFF_VM : OFF_KM) + (size_t)h * NSEG * 2048, NSEG * 2048);
  constexpr unsigned kvd = 256u;
  const int i16 = c, pr = (i16 >> 1) & 1;
  const int sidx = pr ? 0xEEEE : 0x4444;
  const unsigned vM = X_M + sel * 8192 + wave * 1024 + (i16 >> 2) * 256 + (g >> 1) * 128 + (i16 & 1) * 64 + (g & 1) * 32 + pr * 16;
  const int myline = ((a * 4 + wave) * 4 + g) * 2 + (c >> 3);
  const unsigned vPst = (myline >> 1) * 16384 + sel * 8192 + (myline & 1) * 4096 + h * 128 + (c & 7) * 16;
  const int rline = h * 2 + a;
  const unsigned vPld = (rline >> 1) * 16384 + sel * 8192 + (rline & 1) * 4096 + lane * 16;
  const unsigned vMst = ((g < 2) && (c < 8) && wave == 3) ? (unsigned)(X_M + sel * 8192 + rline * 128 + g * 64 + c * 8) : 0x80000000u;
  const bool lo8 = c < 8;
  const float CEXP = 0.08838834764831845f * 1.4426950408889634f;
  const unsigned vKV0 = a ? (unsigned)((2 * wave) * 256 + lane * 4) : (unsigned)(wave * 512 + (c >> 2) * 64 + g * 16 + (c & 3) * 4);
  int qi_st = L_QI + wave * QI_W + (c >> 2) * QI_G + (2 * g) * 16 + (c & 3) * 4;
  asm volatile("" : "+v"(qi_st));
  int qf_ld = L_QI + g * QI_G + (c & 7) * 16;
  asm volatile("" : "+v"(qf_ld));
  const int kf_ld = L_QI + g * QI_G + 256 + (2 * (c >> 2) + (c & 1)) * 16;
  int wo_ld = L_WO + wave * 16384 + lane * 16, wv_ld = L_WV + wave * 12288 + lane * 16;
  asm volatile("" : "+v"(wo_ld), "+v"(wv_ld));
  const int oi_st = L_MST + (wave >> 1) * 1024 + ((2 * (wave & 1) + (c >> 3)) * 16 + 4 * ((c & 7) >> 1) + 2 * (g >> 1) + (c & 1)) * 16 + (g & 1) * 8;

  const char* kbase = ws + OFF_CELL + (size_t)h * 4194304;
  const unsigned fs = sel ^ 1u;
  const char* kvb = kbase + wave * 2048;
  auto kv_go = [&](const char* b, const char* b2, unsigned d, unsigned l16) {
    asm volatile("s_mov_b32 m0, %3\n\ts_nop 0\n\tglobal_load_lds_dwordx4 %0, %1 sc1\n\tglobal_load_lds_dwordx4 %0, %1 offset:1024 sc1\n\ts_add_u32 m0, m0, 0x2000\n\ts_nop 0\n\t"
                 "global_load_lds_dwordx4 %0, %2 sc1\n\tglobal_load_lds_dwordx4 %0, %2 offset:1024 sc1" :: "v"(l16), "s"(b), "s"(b2), "s"(d) : "memory", "scc");
  };
  auto kv_dma = [&](int sg, unsigned l16) { kv_go(kvb + blk(sg * 4, fs), kvb + blk(sg * 4, fs) + 16384, lbase + ((sg & 1) ? L_KV1 : L_KX) + wave * 2048, l16); };
  wait_flags(rX, lane < 4 ? (unsigned)(OFF_FLG + (F_KV + h) * 128 + lane * 4) : 0x80000000u, 1, spin_limit);
  kv_dma(0, lane * 16);
  vmwait<0>();
  int kvknown = 1, kvhave = 1;
  const unsigned l16c = lane * 16;
  int mst_st = L_MST + wave * 1024 + lane * 16, vx_ld = L_VX + wave * 2048 + lane * 16;
  asm volatile("" : "+v"(mst_st), "+v"(vx_ld));
  const unsigned vTS = wave == 3 ? (unsigned)(X_TS + sel * 8192 + lane * 128) : 0x80000000u;
  const unsigned vOne = (lane == 0 && wave == 3) ? (unsigned)(rline * 128) : 0x80000000u;
  const unsigned sTS = X_TS + sel * 8192;
  const unsigned vPz = vPld + wave * 1024;
  if (wave == 3) {
    bf16x8 asel;
#pragma unroll
    for (int j = 0; j < 8; ++j) asel[j] = (__bf16)((c == j) ? 1.0f : 0.0f);
    *(LAS bf16x8*)(lds + L_OI + lane * 16) = asel;
    *(LAS int*)(lds + L_OI + 1028) = 1;
  }
  f32x4 aq0 = zero4(), aq1 = zero4(), ak0 = zero4(), ak1 = zero4(), av0 = zero4(), av1 = zero4();
  lds_barrier();
  bf16x8 kxf[8];
#pragma unroll
  for (int n = 0; n < 8; ++n) kxf[n] = *(LAS bf16x8*)(lds + L_KX + n * 1024 + lane * 16);
  u32x4 f0 = ld128(rX, vM, 0, 16), f1 = ld128(rX, vM, 4096, 16);
  int mst_ld[3];
#pragma unroll
  for (int m = 1; m < 4; ++m) { mst_ld[m - 1] = (mst_st + m * 1024) & 4095; asm volatile("" : "+v"(mst_ld[m - 1])); }
  int kvx_st = L_KVX + (int)vKV0;
  asm volatile("" : "+v"(kvx_st));
  unsigned coff = clock_offset();
#define FENCE() asm volatile("" ::: "memory")
#define KEEP(x) asm volatile("" : "+v"(x))
#define Q0(u) do { if ((u) < 5) SMF_V(aq0, af[u], wq[0][u]); else SMF_A(aq0, af[u], wq[0][u]); } while (0)
#define Q1(u) do { if ((u) < 5) SMF_V(aq1, af[u], wq[1][u]); else SMF_A(aq1, af[u], wq[1][u]); } while (0)
#define QQ(u) do { Q0(u); Q1(u); } while (0)
#define K0(u) SMF_A(ak0, af[u], wk[0][u])
#define K1(u) SMF_A(ak1, af[u], wk[1][u])
#define KK(u) do { K0(u); K1(u); } while (0)
#define V0(u) SMF_A(av0, af[u], wv[0][u])
#define V1(u) SMF_A(av1, af[u], wv[1][u])
#define VV(u) do { V0(u); V1(u); } while (0)
#define LDA(u) do { FENCE(); af[u] = *(LAS bf16x8*)(lds + mst_ld[((u) >> 1) - 1] + ((u) & 1) * 4096); FENCE(); } while (0)
#define LDF(base, n) cat16(*(LAS bf16x8*)(lds + (base) + (n) * 2048), *(LAS bf16x8*)(lds + (base) + (n) * 2048 + 1024))
#define DPPMAX(x, ctl, rm) do { x = max(x, (int)__builtin_amdgcn_update_dpp((int)0x80000000, x, ctl, rm, 0xf, false)); KEEP(x); } while (0)
  for (int s = 0; s < NSEG - 1; ++s) {
    const unsigned sM = (s & 3) * 16384;
    const bool kvnow = (kvhave == s + 2 && kvknown >= s + 3) || s + 2 >= NSEG;
    const char* dsrc = kvb + blk(((kvnow && s + 2 < NSEG) ? s + 2 : s) * 4, fs);
    const char* dsrc2 = dsrc + 16384;
    unsigned ddst = lbase + ((s & 1) ? L_KV1 : L_KX) + wave * 2048;
    asm volatile("" : "+s"(dsrc), "+s"(dsrc2), "+s"(ddst) :: "memory");
    bf16x8 af[8];
    af[0] = asb8(f0); af[1] = asb8(f1);
    *(LAS u32x4*)(lds + mst_st) = f0;
    *(LAS u32x4*)(lds + mst_st + 4096) = f1;
    FENCE();
    Q0(0);
    unsigned mn = min3u(f0.x, f0.y, f0.z); KEEP(mn);
    __builtin_amdgcn_raw_buffer_store_b128(__builtin_bit_cast(u32x4, av0), rX, vPz, ((s + 1) & 1) * 524288, 16);
    FENCE();
    Q1(0);
    mn = min3u(mn, f0.w, f1.x); KEEP(mn);
    __builtin_amdgcn_raw_buffer_store_b64(__builtin_bit_cast(u32x2, __builtin_shufflevector(av0, av0, 0, 1)), rX, vMst, ((s + 2) & 3) * 16384, 16);
    FENCE();
    Q0(1);
    mn = min(min3u(mn, f1.y, f1.z), f1.w); KEEP(mn);
    Q1(1);
    if (__builtin_expect(__builtin_amdgcn_ballot_w64(mn == 0) != 0, 0)) {
      for (int spin = 0; spin < spin_limit; ++spin) {
        reld2(rX, vM, sM, sM + 4096, f0, f1);
        if (__builtin_amdgcn_ballot_w64(min(min4u(f0), min4u(f1)) == 0) == 0) break;
        if (spin == spin_limit - 1) spin_limit = 0;
      }
      af[0] = asb8(f0); af[1] = asb8(f1);
      *(LAS u32x4*)(lds + mst_st) = f0;
      *(LAS u32x4*)(lds + mst_st + 4096) = f1;
      aq0 = zero4(); aq1 = zero4();
      asm volatile("s_nop 1" : "+v"(aq0), "+v"(aq1), "+v"(af[0]), "+v"(af[1]));
      QQ(0); QQ(1);
    }
    lds_barrier();
    unsigned long long mt64;
    asm volatile("s_memtime %0" : "=s"(mt64) :: "memory");
    bf16x16 wl[6];
    LDA(2); LDA(3); K0(0); LDA(4); LDA(5); K1(0); LDA(6); LDA(7); K0(1); K1(1);
    V0(0);
    __builtin_amdgcn_raw_buffer_store_b32((unsigned)s, rX, vOne, OFF_FLG + F_CH * 128, 16);
    FENCE();
    V1(0); V0(1);
    asm volatile("s_waitcnt lgkmcnt(0)" : "+s"(mt64) :: "memory");
    const unsigned mb1 = (unsigned)mt64;
    const unsigned rown = mb1 + coff;
    __builtin_amdgcn_raw_buffer_store_b32(rown, rX, vOne, sTS + sM, 16);
    FENCE();
    V1(1);
    QQ(2); FENCE(); wl[0] = LDF(wv_ld, 0); FENCE();
    QQ(3); FENCE(); wl[1] = LDF(wv_ld, 1); FENCE();
    QQ(4); FENCE(); wl[2] = LDF(wv_ld, 2); FENCE();
    QQ(5); FENCE(); wl[3] = LDF(wv_ld, 3); FENCE();
    QQ(6); QQ(7);
    K0(2); K1(2); K0(3);
    asm volatile("s_nop 0" : "+v"(aq0), "+v"(aq1));
    {
      float t0 = aq0[0] + aq0[2], t1 = aq1[0] + aq1[2]; KEEP(t0); KEEP(t1);
      K1(3);
      float t2 = aq0[1] + aq0[3], t3 = aq1[1] + aq1[3]; KEEP(t2); KEEP(t3);
      K0(4);
      const unsigned d0 = pk2(t0, t1), d1 = pk2(t2, t3);
      *(LAS unsigned*)(lds + qi_st) = d0; *(LAS unsigned*)(lds + qi_st + 16) = d1;
      FENCE();
    }
    K1(4); K0(5); K1(5); K0(6);
    lds_barrier();
    bf16x8 qf[4], kf[4];
    qf[0] = *(LAS bf16x8*)(lds + qf_ld); qf[1] = *(LAS bf16x8*)(lds + qf_ld + QI_W); FENCE();
    K1(6); FENCE();
    qf[2] = *(LAS bf16x8*)(lds + qf_ld + 2 * QI_W); qf[3] = *(LAS bf16x8*)(lds + qf_ld + 3 * QI_W); FENCE();
    K0(7); FENCE(); wl[4] = LDF(wv_ld, 4); FENCE();
    K1(7); FENCE(); wl[5] = LDF(wv_ld, 5); FENCE();
    V0(2); V1(2); V0(3);
    asm volatile("" : "+v"(ak0), "+v"(ak1));
    unsigned kd0, kd1;
    f32x4 sx0, sx1, sm;
    {
      float t0 = ak0[0] + ak0[2], t1 = ak1[0] + ak1[2]; KEEP(t0); KEEP(t1);
      V1(3);
      float t2 = ak0[1] + ak0[3], t3 = ak1[1] + ak1[3]; KEEP(t2); KEEP(t3);
      MF0m(sx0, kxf[0], qf[0]);
      kd0 = pk2(t0, t1); kd1 = pk2(t2, t3);
      *(LAS unsigned*)(lds + qi_st + 256) = kd0; *(LAS unsigned*)(lds + qi_st + 256 + 16) = kd1;
    }
    MF0m(sx1, kxf[1], qf[0]);
    MFm(sx0, kxf[2], qf[1]);
    int ts = (int)__builtin_amdgcn_raw_buffer_load_b32(rX, vTS, (s & 3) * 16384, 16);
    MFm(sx1, kxf[3], qf[1]);
    MFm(sx0, kxf[4], qf[2]); MFm(sx1, kxf[5], qf[2]);
    lds_barrier();
    kf[0] = *(LAS bf16x8*)(lds + kf_ld); kf[1] = *(LAS bf16x8*)(lds + kf_ld + QI_W);
    MFm(sx0, kxf[6], qf[3]);
    kf[2] = *(LAS bf16x8*)(lds + kf_ld + 2 * QI_W); kf[3] = *(LAS bf16x8*)(lds + kf_ld + 3 * QI_W);
    MFm(sx1, kxf[7], qf[3]);
    const bf16x8 vx0 = *(LAS bf16x8*)(lds + vx_ld), vx1 = *(LAS bf16x8*)(lds + vx_ld + 1024);
    FENCE();
    bf16x16 wo[8];
    SMF_V(av0, af[6], wl[0]); SMF_V(av1, af[6], wl[1]); FENCE();
    wo[0] = LDF(wo_ld, 0);
    MF0m(sm, kf[0], qf[0]);
    wo[1] = LDF(wo_ld, 1);
    MFm(sm, kf[1], qf[1]);
    float mm = max3f(sx0[0], sx0[1], sx0[2]), m2 = max3f(sx0[3], sx1[0], sx1[1]); KEEP(mm); KEEP(m2);
    MFm(sm, kf[2], qf[2]);
    m2 = max3f(m2, sx1[2], sx1[3]); mm = max2f(mm, m2); KEEP(mm);
    MFm(sm, kf[3], qf[3]);
    u32x2 on_lo, on_hi;
    asm volatile("v_mov_b64 %0, %2\n\tv_mov_b64 %1, %2" : "=v"(on_lo), "=v"(on_hi) : "s"(0x3f803f803f803f80ull));
    const bf16x8 ones = asb8(__builtin_shufflevector(on_lo, on_hi, 0, 1, 2, 3));
    wo[4] = LDF(wo_ld, 4); FENCE();
    SMF_V(av0, af[7], wl[2]); FENCE(); wo[5] = LDF(wo_ld, 5); FENCE(); SMF_V(av1, af[7], wl[3]);
    SMF_V(av0, af[5], wl[4]);
    asm volatile("" : "+v"(sm));
    mm = max3f(mm, sm[0], sm[1]);
    { float t_;
      asm volatile("v_mov_b32 %1, %0\n\tv_smfmac_f32_16x16x64_bf16 %2, %4, %5, %8\n\ts_nop 0\n\tv_permlane32_swap_b32 %0, %1\n\tv_max_f32 %0, %0, %1\n\tv_mov_b32 %1, %0\n\t"
                   "v_smfmac_f32_16x16x64_bf16 %3, %6, %7, %8\n\ts_nop 0\n\tv_permlane16_swap_b32 %0, %1\n\tv_max_f32 %0, %0, %1"
                   : "+v"(mm), "=&v"(t_), "+v"(av1), "+v"(av0) : "v"(af[5]), "v"(wl[5]), "v"(af[4]), "a"(wv[0][4]), "v"(sidx)); }
    V1(4);
    float mc = mm * CEXP; KEEP(mc);
    float ex[8];
#pragma unroll
    for (int j = 0; j < 4; ++j) { ex[j] = __builtin_amdgcn_exp2f(sx0[j] * CEXP - mc); ex[4 + j] = __builtin_amdgcn_exp2f(sx1[j] * CEXP - mc); }
    const u32x4 exp_ = {pk2(ex[0], ex[1]), pk2(ex[2], ex[3]), pk2(ex[4], ex[5]), pk2(ex[6], ex[7])};
    f32x4 ax, dn;
    u32x2 vv;
    { const bf16x8 b0 = asb8((u32x4){lo8 ? exp_.x : 0u, lo8 ? exp_.y : 0u, lo8 ? exp_.z : 0u, lo8 ? exp_.w : 0u});
      const bf16x8 ea = asb8(exp_);
      MFN0(ax, vx0, b0);
      float e0 = __builtin_amdgcn_exp2f(sm[0] * CEXP - mc); KEEP(e0);
      MF0(dn, ones, ea);
      float e1 = __builtin_amdgcn_exp2f(sm[1] * CEXP - mc); KEEP(e1);
      const bf16x8 b1 = asb8((u32x4){lo8 ? 0u : exp_.x, lo8 ? 0u : exp_.y, lo8 ? 0u : exp_.z, lo8 ? 0u : exp_.w});
      MFN(ax, vx1, b1);
      asm volatile("" : "+v"(av0), "+v"(av1));
      const unsigned em = pk2(e0, e1);
      { const f32x2 y0 = fold(av0), y1 = fold(av1); vv = (u32x2){pk2(y0.x, y0.y), pk2(y1.x, y1.y)}; }
      const u32x2 bm = {lo8 ? em : 0u, lo8 ? 0u : em};
      const bf16x4 on2 = __builtin_shufflevector(ones, ones, 0, 1, 2, 3);
      asm volatile("s_nop 1\n\tv_mfma_f32_16x16x16_bf16 %0, %2, %4, %0\n\tv_mfma_f32_16x16x16_bf16 %1, %3, %4, %1" : "+v"(dn), "+v"(ax) : "v"(on2), "v"(vv), "v"(bm) : "memory"); }
    f32x4 ap0 = zero4(), ap1 = zero4(), ap2 = zero4();
    asm volatile("s_nop 0" : "+v"(dn), "+v"(ax) :: "memory");
    const float rinv = __builtin_amdgcn_rcpf(dn[0]);
    {
      const u32x2 o = {pk2(ax[0] * rinv, ax[1] * rinv), pk2(ax[2] * rinv, ax[3] * rinv)};
      *(LAS u32x2*)(lds + oi_st) = o;
    }
    FENCE();
    *(LAS unsigned*)(lds + kvx_st) = a ? vv.x : kd0; *(LAS unsigned*)(lds + kvx_st + 256) = a ? vv.y : kd1;
    FENCE();
    asm volatile("" : "+v"(ts) :: "memory");
    asm volatile("s_cmp_lg_u32 %4, 3\n\ts_cselect_b64 exec, -1, 0\n\ts_mov_b32 m0, %3\n\ts_nop 0\n\tglobal_load_lds_dwordx4 %0, %1 sc1\n\tglobal_load_lds_dwordx4 %0, %1 offset:1024 sc1\n\ts_add_u32 m0, m0, 0x2000\n\ts_nop 0\n\t"
                 "global_load_lds_dwordx4 %0, %2 sc1\n\tglobal_load_lds_dwordx4 %0, %2 offset:1024 sc1\n\ts_mov_b64 exec, -1"
                 :: "v"(l16c), "s"(dsrc), "s"(dsrc2), "s"(ddst), "s"(wave) : "memory", "scc");
    f32x4 ap3 = zero4();
    lds_barrier();
    const bf16x8 o0 = *(LAS bf16x8*)(lds + L_MST + lane * 16), o1 = *(LAS bf16x8*)(lds + L_MST + 1024 + lane * 16);
    FENCE();
    wo[2] = LDF(wo_ld, 2); wo[3] = LDF(wo_ld, 3); wo[6] = LDF(wo_ld, 6); wo[7] = LDF(wo_ld, 7);
    FENCE();
    SMF_V(ap0, o0, wo[0]); SMF_V(ap1, o0, wo[1]); SMF_V(ap0, o1, wo[4]); SMF_V(ap1, o1, wo[5]);
    SMF_V(ap2, o0, wo[2]); SMF_V(ap3, o0, wo[3]); SMF_V(ap2, o1, wo[6]);
    asm volatile("" : "+v"(ap0), "+v"(ap1));
    u32x4 pv;
    { f32x2 y0 = fold(ap0), y1 = fold(ap1); KEEP(y0); KEEP(y1);
      SMF_V(ap3, o1, wo[7]); KEEP(y0);
      pv.x = max(pk2(y0.x, y1.x), 1u); pv.z = max(pk2(y0.y, y1.y), 1u); }
    asm volatile("" : "+v"(pv.x), "+v"(pv.z), "+v"(ap2));
    { f32x2 y2 = fold(ap2);
      asm volatile("s_nop 1" : "+v"(y2), "+v"(ap3));
      const f32x2 y3 = fold(ap3);
      pv.y = max(pk2(y2.x, y3.x), 1u); pv.w = max(pk2(y2.y, y3.y), 1u); }
    const unsigned sP = (s & 1) * 524288;
    st128(pv, rX, vPst, sP);
    if (wave == 3) {
      asm volatile("v_subrev_u32 %0, %1, %0\n\tv_min_u32 %0, 0x800, %0\n\tv_and_b32 %0, 0x7ff, %0" : "+v"(ts) : "s"(rown));
      DPPMAX(ts, 0x111, 0xf); DPPMAX(ts, 0x112, 0xf); DPPMAX(ts, 0x114, 0xf); DPPMAX(ts, 0x118, 0xf); DPPMAX(ts, 0x142, 0xa); DPPMAX(ts, 0x143, 0xc);
      const int tsm = __builtin_amdgcn_readlane(ts, 63);
      const unsigned tbase = mb1 + (unsigned)tsm;
      const bf16x8 asel = *(LAS bf16x8*)(lds + L_OI + lane * 16);
      FENCE();
      wait_until(tbase + CA);
      u32x4 r0 = ld128(rX, vPld, sP, 16), r1 = ld128(rX, vPld, sP + 1024, 16), r2 = ld128(rX, vPld, sP + 2048, 16), r3 = ld128(rX, vPld, sP + 3072, 16);
      f32x4 d0;
      unsigned rm;
      { const bf16x8 b0 = asb8(r0), b1 = asb8(r1), b2 = asb8(r2), b3 = asb8(r3);
        MF0(d0, asel, b0); rm = min4u(r0); KEEP(rm);
        MF(d0, asel, b1); rm = min3u(min3u(rm, r1.x, r1.y), r1.z, r1.w); KEEP(rm);
        MF(d0, asel, b2); rm = min3u(min3u(rm, r2.x, r2.y), r2.z, r2.w); KEEP(rm);
        MF(d0, asel, b3); rm = min3u(min3u(rm, r3.x, r3.y), r3.z, r3.w); KEEP(rm); }
      if (__builtin_expect(__builtin_amdgcn_ballot_w64(rm == 0) != 0, 0)) {
        for (int spin = 0; spin < spin_limit; ++spin) {
          reld4(rX, vPld, sP, r0, r1, r2, r3);
          if (__builtin_amdgcn_ballot_w64(min(min3u(min4u(r0), min4u(r1), min4u(r2)), min4u(r3)) == 0) == 0) break;
          if (spin == spin_limit - 1) spin_limit = 0;
        }
        const bf16x8 b0 = asb8(r0), b1 = asb8(r1), b2 = asb8(r2), b3 = asb8(r3);
        MFN0(d0, asel, b0); MF(d0, asel, b1); MF(d0, asel, b2); MF(d0, asel, b3);
        PIN1(7, d0);
      }
      PIN1(3, d0);
#pragma unroll
      for (int j = 0; j < 4; ++j) {
        float dj = d0[j];
        asm("v_add_f32_dpp %0, %0, %0 row_ror:8 row_mask:0xf bank_mask:0xf" : "+v"(dj));
        d0[j] = dj;
      }
      u32x2 mo = {max(pk2(d0[0], d0[1]), 1u), max(pk2(d0[2], d0[3]), 1u)};
      __builtin_amdgcn_raw_buffer_store_b64(mo, rX, vMst, ((s + 1) & 3) * 16384, 16);
      FENCE();
      wait_until(tbase + CB);
    } else if (wave == 0) {
      kv_go(dsrc + 6144, dsrc2 + 6144, ddst + 6144, l16c);
      if (kvknown < s + 6 && kvknown < NSEG) {
        const int v = (int)__builtin_amdgcn_raw_buffer_load_b32(rX, (unsigned)(OFF_FLG + (F_KV + h) * 128) + ((l16c >> 2) & 12u), 0, 16);
        const int k_ = min(min(__builtin_amdgcn_readlane(v, 0), __builtin_amdgcn_readlane(v, 1)), min(__builtin_amdgcn_readlane(v, 2), __builtin_amdgcn_readlane(v, 3)));
        *(LAS int*)(lds + L_OI + 1028) = k_;
      }
    } else {
      st128(*(LAS u32x4*)(lds + L_KVX + (wave - 1) * 1024 + l16c), rKVM, l16c, s * 2048 + (wave - 1) * 1024);
      if (wave == 1) { const unsigned o_ = clock_offset(); *(LAS unsigned*)(lds + L_OI + 1024) = o_; }
    }
    if (__builtin_expect(!kvnow && wave != 3, 0)) {
      const unsigned fo = (unsigned)(OFF_FLG + (F_KV + h) * 128) + ((l16c >> 2) & 12u);
      if (kvhave == s + 1) {
        wait_flags(rX, fo, s + 2, spin_limit);
        kv_dma(s + 1, l16c);
        if (wave == 0) kv_go(kvb + blk((s + 1) * 4, fs) + 6144, kvb + blk((s + 1) * 4, fs) + 16384 + 6144, lbase + (((s + 1) & 1) ? L_KV1 : L_KX) + 6144, l16c);
        vmwait<0>();
        kvhave = s + 2;
      }
      if (__builtin_amdgcn_ballot_w64(__builtin_amdgcn_raw_buffer_load_b32(rX, fo, 0, 16) < (unsigned)(s + 3)) == 0) {
        kv_dma(s + 2, l16c);
        if (wave == 0) kv_go(kvb + blk((s + 2) * 4, fs) + 6144, kvb + blk((s + 2) * 4, fs) + 16384 + 6144, lbase + ((s & 1) ? L_KV1 : L_KX) + 6144, l16c);
        kvhave = s + 3;
      }
    } else kvhave = s + 3;
    lds_barrier();
    f0 = ld128(rX, vM, ((s + 1) & 3) * 16384, 16); f1 = ld128(rX, vM, ((s + 1) & 3) * 16384 + 4096, 16);
    FENCE();
    aq0 = zero4(); aq1 = zero4(); ak0 = zero4(); ak1 = zero4(); av0 = zero4(); av1 = zero4();
#pragma unroll
    for (int n = 0; n < 8; ++n) kxf[n] = *(LAS bf16x8*)(lds + ((s & 1) ? L_KX : L_KV1) + n * 1024 + lane * 16);
    vx_ld += (s & 1) ? (L_KX - L_KV1) : (L_KV1 - L_KX);
    { const u32x2 cv = *(LAS u32x2*)(lds + L_OI + 1024); coff = __builtin_amdgcn_readfirstlane(cv.x); kvknown = __builtin_amdgcn_readfirstlane(cv.y); }
    FENCE();
  }
  {
    const unsigned sM = ((NSEG - 1) & 3) * 16384;
    for (int spin = 0; __builtin_amdgcn_ballot_w64(min(min4u(f0), min4u(f1)) == 0) != 0 && spin < spin_limit; ++spin) reld2(rX, vM, sM, sM + 4096, f0, f1);
    *(LAS u32x4*)(lds + mst_st) = f0;
    *(LAS u32x4*)(lds + mst_st + 4096) = f1;
    bf16x16 wl[6];
#pragma unroll
    for (int n = 0; n < 6; ++n) wl[n] = LDF(wv_ld, n);
    lds_barrier();
    __builtin_amdgcn_raw_buffer_store_b32((unsigned)(NSEG - 1), rX, vOne, OFF_FLG + F_CH * 128, 16);
    bf16x8 af[8];
#pragma unroll
    for (int u = 0; u < 8; ++u) af[u] = *(LAS bf16x8*)(lds + ((mst_st + (u >> 1) * 1024) & 4095) + (u & 1) * 4096);
    unsigned o0, o1;
    if (a == 0) {
      KK(0); KK(1); KK(2); KK(3); KK(4); KK(5); KK(6); KK(7);
      PIN2(7, ak0, ak1);
      o0 = pk2(ak0[0] + ak0[2], ak1[0] + ak1[2]); o1 = pk2(ak0[1] + ak0[3], ak1[1] + ak1[3]);
    } else {
      VV(0); VV(1); VV(2); VV(3); VV(4);
      SMF_V(av0, af[6], wl[0]); SMF_V(av1, af[6], wl[1]); SMF_V(av0, af[7], wl[2]); SMF_V(av1, af[7], wl[3]); SMF_V(av0, af[5], wl[4]); SMF_V(av1, af[5], wl[5]);
      PIN2(7, av0, av1);
      o0 = pk2(av0[0] + av0[2], av0[1] + av0[3]); o1 = pk2(av1[0] + av1[2], av1[1] + av1[3]);
    }
    __builtin_amdgcn_raw_buffer_store_b32(o0, rKVM, kvx_st - L_KVX, (NSEG - 1) * 2048, 16);
    __builtin_amdgcn_raw_buffer_store_b32(o1, rKVM, kvx_st - L_KVX, (NSEG - 1) * 2048 + 256, 16);
    vmwait<0>();
    lds_barrier();
    __builtin_amdgcn_raw_buffer_store_b32((unsigned)NSEG, rX, vOne, OFF_FLG + F_CH * 128, 16);
  }
}

#define SD1 3
#define SD2 5
__device__ __forceinline__ void start_delay(int us) { for (int i = 0; i < us; ++i) __builtin_amdgcn_s_sleep(37); }
__global__ void __launch_bounds__(256, 1) k_main(Params p) {
  extern __shared__ __attribute__((aligned(16))) char smem[];
  LAS char* lds = (LAS char*)smem;
  const int b = blockIdx.x, x = b & 7, slot = b >> 3;
  int qi = -1;
  if (x < 4) {
    if (slot < 16) chain_role(p, lds, x * 8 + (slot >> 1), slot & 1);
    else if (slot < 18) { qi = 8 + x * 2 + slot - 16; start_delay(SD2); }
    else if (slot < 22) {
      if (b == 21 * 8 + 3) init_role(p);
      for (int i = (slot - 18) * 4 + x; i < NSEG; i += 16) ln_role(p, lds, i);
    } else {
      warm_cols<40>(p.Wq, x, slot - 22); warm_cols<40>(p.Wk, x, slot - 22); warm_cols<40>(p.Wv, x, slot - 22);
      warm<26>((const char*)p.Wo + (size_t)x * 1048576, 1048576, (slot - 22) * 106496);
    }
  } else {
    const int y = x - 4;
    if (slot >= 16 && slot < 26) { warm_cols<40>(p.Wk, y, slot - 16); warm_cols<40>(p.Wv, y, slot - 16); }
    if (slot >= 16 && slot < 30) { const int tile = y * 14 + slot - 16; warm<37>((const char*)p.w_sp + (size_t)(tile / 7) * 1048576, 1048576, (tile % 7) * 151552); }
    else
    if (slot >= 8) start_delay(slot < 30 ? SD1 : SD2);
    if (slot < 8) {
      proj_role<0, 4>(p, lds, y * 8 + slot, 0, NSEG, F_KV + y * 8 + slot);
      ah_role(p, lds, y * 8 + slot, S_SW + 1, F_AH2 + y * 8 + slot);
    } else if (slot < 16) {
      const int i = y * 8 + slot - 8;
      if (i < 28) proj_role<2, 2>(p, lds, i % 7, (i / 7) * 16, (i / 7) * 16 + 16, F_Z1 + i);
      else warm<56>(p.w_out, FH * DM * 2, (i - 28) * 229376);
      ah_role(p, lds, i, 0, F_AH + i);
    } else if (slot < 30) {
      const int tile = y * 14 + slot - 16;
      sp_role(p, lds, tile);
      lds_barrier();
      if (slot < 24) red_role(p, lds, y * 8 + slot - 16);
    } else qi = y * 2 + slot - 30;
  }
  if (qi >= 0) proj_role<1, 4>(p, lds, qi, 0, NSEG, F_Q + qi);
}

extern "C" void kernel_launch(void* const* d_in, const int* in_sizes, int n_in, void* d_out, int out_size, void* d_ws, size_t ws_size,
                              hipStream_t stream) {
  Params p;
  p.x = (const u16*)d_in[0]; p.mem0 = (const u16*)d_in[1]; p.Wq = (const u16*)d_in[2]; p.Wk = (const u16*)d_in[3];
  p.Wv = (const u16*)d_in[4]; p.Wo = (const u16*)d_in[5]; p.w_in = (const u16*)d_in[8]; p.w_sp = (const u16*)d_in[9];
  p.b_sp = (const u16*)d_in[10]; p.w_out = (const u16*)d_in[11]; p.out = (u16*)d_out;
  p.ws = (char*)(((uintptr_t)d_ws + (2u << 20) - 1) & ~(uintptr_t)((2u << 20) - 1));
  (void)hipFuncSetAttribute((const void*)k_main, hipFuncAttributeMaxDynamicSharedMemorySize, 163840);
  (void)hipMemsetAsync(p.ws + OFF_X, 0, X_ZERO, stream);
  hipLaunchKernelGGL(k_main, dim3(256), dim3(256), 163840, stream, p);
}
```
